```python
import torch

def _softmax(x, axis=-1):
    return torch.softmax(x, dim=axis)
def _gelu(x):
    return torch.nn.functional.gelu(x, approximate="tanh")
def _silu(x):
    return torch.nn.functional.silu(x)
def _sigmoid(x):
    return torch.sigmoid(x)
def _layernorm(x, axis=-1, eps=1e-5):
    m = x.mean(dim=axis, keepdim=True)
    v = x.var(dim=axis, keepdim=True, unbiased=False)
    return (x - m) / torch.sqrt(v + eps)
def _scatter_add(tgt, idx, src, axis=0):
    return tgt.clone().index_add_(axis, idx, src)
def _segment_sum(src, seg, n):
    out = torch.zeros((n,) + src.shape[1:], dtype=src.dtype)
    return out.index_add_(0, seg, src)
def _pad(x, pw):
    flat = tuple(v for pair in reversed(pw) for v in pair)
    return torch.nn.functional.pad(x, flat)

kernel_name = 'standalone__geglu_ffn__prompt'

def setup_inputs():

    _w = (lambda a: (lambda t: t.to(torch.bfloat16) if t.is_floating_point() else t)(torch.as_tensor(a)))
    B, S, D, F = 1, 2048, 512, 1792
    return {
        "x": _w((lambda *s: torch.randn(*s, dtype=torch.bfloat16) * 0.05)(B, S, D)),
        "w_gate": _w((lambda *s: torch.randn(*s, dtype=torch.bfloat16) * 0.05)(D, F)),
        "w_up": _w((lambda *s: torch.randn(*s, dtype=torch.bfloat16) * 0.05)(D, F)),
        "w_down": _w((lambda *s: torch.randn(*s, dtype=torch.bfloat16) * 0.05)(F, D)),
    }

def reference(x, w_gate, w_up, w_down):
    return (_gelu(x @ w_gate) * (x @ w_up)) @ w_down
```

```cpp
#include <hip/hip_runtime.h>
#include <hip/hip_bf16.h>
typedef __attribute__((ext_vector_type(8))) short bf16x8;
typedef __attribute__((ext_vector_type(4))) float f32x4;
constexpr int S=2048, D=512, F=1792;
__device__ __forceinline__ float bf2f(unsigned short b){ return __builtin_bit_cast(float,(unsigned)b<<16);}
__device__ __forceinline__ unsigned short f2bf(float f){ __hip_bfloat16 h = __float2bfloat16(f); return __builtin_bit_cast(unsigned short,h);}
__global__ void k1(const unsigned short* x, const unsigned short* wg, const unsigned short* wu, unsigned short* h){
  int wave = (blockIdx.x*blockDim.x+threadIdx.x)>>6; int lane=threadIdx.x&63;
  int tm = wave/(F/16), tn = wave%(F/16);
  f32x4 g={0,0,0,0}, u={0,0,0,0};
  for(int k0=0;k0<D;k0+=32){
    bf16x8 a = *(const bf16x8*)(x + (tm*16+(lane&15))*D + k0 + 8*(lane>>4));
    bf16x8 bg, bu;
    for(int j=0;j<8;j++){ bg[j]=wg[(k0+8*(lane>>4)+j)*F + tn*16+(lane&15)]; bu[j]=wu[(k0+8*(lane>>4)+j)*F + tn*16+(lane&15)]; }
    g=__builtin_amdgcn_mfma_f32_16x16x32_bf16(a,bg,g,0,0,0);
    u=__builtin_amdgcn_mfma_f32_16x16x32_bf16(a,bu,u,0,0,0);
  }
  for(int r=0;r<4;r++){
    float gv=g[r]; float z=0.7978845608028654f*(gv+0.044715f*gv*gv*gv);
    float ge = gv/(1.f+__expf(-2.f*z));
    h[(tm*16+(lane>>4)*4+r)*F + tn*16+(lane&15)] = f2bf(ge*u[r]);
  }
}
__global__ void k2(const unsigned short* h, const unsigned short* wd, unsigned short* out){
  int wave = (blockIdx.x*blockDim.x+threadIdx.x)>>6; int lane=threadIdx.x&63;
  int tm = wave/(D/16), tn = wave%(D/16);
  f32x4 g={0,0,0,0};
  for(int k0=0;k0<F;k0+=32){
    bf16x8 a = *(const bf16x8*)(h + (tm*16+(lane&15))*F + k0 + 8*(lane>>4));
    bf16x8 b;
    for(int j=0;j<8;j++){ b[j]=wd[(k0+8*(lane>>4)+j)*D + tn*16+(lane&15)]; }
    g=__builtin_amdgcn_mfma_f32_16x16x32_bf16(a,b,g,0,0,0);
  }
  for(int r=0;r<4;r++) out[(tm*16+(lane>>4)*4+r)*D + tn*16+(lane&15)] = f2bf(g[r]);
}
extern "C" void kernel_launch(void* const* d_in, const int* in_sizes, int n_in, void* d_out, int out_size, void* d_ws, size_t ws_size, hipStream_t stream) {
  const unsigned short* x=(const unsigned short*)d_in[0]; const unsigned short* wg=(const unsigned short*)d_in[1];
  const unsigned short* wu=(const unsigned short*)d_in[2]; const unsigned short* wd=(const unsigned short*)d_in[3];
  unsigned short* h=(unsigned short*)d_ws;
  k1<<<(S/16)*(F/16)/4,256,0,stream>>>(x,wg,wu,h);
  k2<<<(S/16)*(D/16)/4,256,0,stream>>>(h,wd,(unsigned short*)d_out);
}
```

```cpp
#include <hip/hip_runtime.h>
#include <hip/hip_bf16.h>
typedef __attribute__((ext_vector_type(8))) short bf16x8;
typedef __attribute__((ext_vector_type(4))) float f32x4;
constexpr int S=2048, D=512, F=1792;
__device__ __forceinline__ float bf2f(unsigned short b){ return __builtin_bit_cast(float,(unsigned)b<<16);}
__device__ __forceinline__ unsigned short f2bf(float f){ __hip_bfloat16 h = __float2bfloat16(f); return __builtin_bit_cast(unsigned short,h);}
__global__ void k1(const unsigned short* x, const unsigned short* wg, const unsigned short* wu, unsigned short* h){
  int wave = (blockIdx.x*blockDim.x+threadIdx.x)>>6; int lane=threadIdx.x&63;
  int tm = wave/(F/16), tn = wave%(F/16);
  f32x4 g={0,0,0,0}, u={0,0,0,0};
  for(int k0=0;k0<D;k0+=32){
    bf16x8 a = *(const bf16x8*)(x + (tm*16+(lane&15))*D + k0 + 8*(lane>>4));
    bf16x8 bg, bu;
    for(int j=0;j<8;j++){ bg[j]=wg[(k0+8*(lane>>4)+j)*F + tn*16+(lane&15)]; bu[j]=wu[(k0+8*(lane>>4)+j)*F + tn*16+(lane&15)]; }
    g=__builtin_amdgcn_mfma_f32_16x16x32_bf16(a,bg,g,0,0,0);
    u=__builtin_amdgcn_mfma_f32_16x16x32_bf16(a,bu,u,0,0,0);
  }
  for(int r=0;r<4;r++){
    float gv=g[r]; float z=0.7978845608028654f*(gv+0.044715f*gv*gv*gv);
    float ge = gv/(1.f+__expf(-2.f*z));
    h[(tm*16+(lane>>4)*4+r)*F + tn*16+(lane&15)] = f2bf(ge*u[r]);
  }
}
__global__ void k2(const unsigned short* h, const unsigned short* wd, unsigned short* out){
  int wave = (blockIdx.x*blockDim.x+threadIdx.x)>>6; int lane=threadIdx.x&63;
  int tm = wave/(D/16), tn = wave%(D/16);
  f32x4 g={0,0,0,0};
  for(int k0=0;k0<F;k0+=32){
    bf16x8 a = *(const bf16x8*)(h + (tm*16+(lane&15))*F + k0 + 8*(lane>>4));
    bf16x8 b;
    for(int j=0;j<8;j++){ b[j]=wd[(k0+8*(lane>>4)+j)*D + tn*16+(lane&15)]; }
    g=__builtin_amdgcn_mfma_f32_16x16x32_bf16(a,b,g,0,0,0);
  }
  for(int r=0;r<4;r++) out[(tm*16+(lane>>4)*4+r)*D + tn*16+(lane&15)] = f2bf(g[r]);
}

__global__ void pad(unsigned* sink){
  unsigned v=threadIdx.x;
  asm volatile(".rept 1\n v_add3_u32 %0, %0, %0, %0\n .endr" : "+v"(v));
  if(v==12345) sink[0]=v;
}
extern "C" void kernel_launch(void* const* d_in, const int* in_sizes, int n_in, void* d_out, int out_size, void* d_ws, size_t ws_size, hipStream_t stream) {
  const unsigned short* x=(const unsigned short*)d_in[0]; const unsigned short* wg=(const unsigned short*)d_in[1];
  const unsigned short* wu=(const unsigned short*)d_in[2]; const unsigned short* wd=(const unsigned short*)d_in[3];
  unsigned short* h=(unsigned short*)d_ws;
  pad<<<256,256,0,stream>>>((unsigned*)((char*)d_ws+(128<<20)));
  k1<<<(S/16)*(F/16)/4,256,0,stream>>>(x,wg,wu,h);
  k2<<<(S/16)*(D/16)/4,256,0,stream>>>(h,wd,(unsigned short*)d_out);
}
```

```cpp
#include <hip/hip_runtime.h>
#include <hip/hip_bf16.h>

typedef short s16x4 __attribute__((ext_vector_type(4)));
typedef short bf16x8 __attribute__((ext_vector_type(8)));
typedef __bf16 bf16x2 __attribute__((ext_vector_type(2)));
typedef float f32x2 __attribute__((ext_vector_type(2)));
typedef float f32x4 __attribute__((ext_vector_type(4)));
typedef float f32x16 __attribute__((ext_vector_type(16)));
typedef unsigned u32x2 __attribute__((ext_vector_type(2)));
typedef unsigned u32x4 __attribute__((ext_vector_type(4)));
#define LAS __attribute__((address_space(3)))
#define DI __device__ __forceinline__

constexpr int S = 2048, D = 512, F = 1792;
constexpr int LDS_BYTES = 160 * 1024;
constexpr int SLOT = 48 * 1024;
constexpr int SPARE = 3 * SLOT;
constexpr unsigned H_BYTES = S * F * 2;
constexpr unsigned FLAG_OFF = 8u << 20;
constexpr unsigned FLAG_BYTES = 1024 * 128;

#define ROT_MODE 1

#define STAMP(i)

DI u32x4 mksrd(const void* p, unsigned bytes) {
  unsigned long long a = (unsigned long long)p;
  u32x4 r = {(unsigned)a, (unsigned)(a >> 32) & 0xffffu, bytes, 0x00020000u};
  return r;
}
#define WAITV(n) asm volatile("s_waitcnt vmcnt(" #n ")" ::: "memory")
#define WAITL0() asm volatile("s_waitcnt lgkmcnt(0)" ::: "memory")
#define BAR() asm volatile("s_barrier" ::: "memory")

template <int N, int GS>
DI void dma_run(unsigned lds, unsigned v0, unsigned v1, u32x4 srd, unsigned soff) {
  asm volatile(
      "s_mov_b32 m0, %1\n\t"
      "s_nop 0\n\t"
      ".rept %5\n\t"
      "buffer_load_dwordx4 %2, %4, %0 offen lds\n\t"
      "s_add_u32 m0, m0, 1024\n\t"
      "s_add_u32 %0, %0, %6\n\t"
      "buffer_load_dwordx4 %3, %4, %0 offen lds\n\t"
      "s_add_u32 m0, m0, 1024\n\t"
      "s_add_u32 %0, %0, %6\n\t"
      ".endr"
      : "+s"(soff)
      : "s"(lds), "v"(v0), "v"(v1), "s"(srd), "i"(N / 2), "i"(GS)
      : "memory", "scc");
}

DI s16x4 trd(unsigned a) { return __builtin_amdgcn_ds_read_tr16_b64_v4i16((LAS s16x4*)a); }
DI bf16x8 cat(s16x4 a, s16x4 b) { return __builtin_shufflevector(a, b, 0, 1, 2, 3, 4, 5, 6, 7); }
#define MFMA(a, b, c) __builtin_amdgcn_mfma_f32_32x32x16_bf16((a), (b), (c), 0, 0, 0)

DI unsigned pk(float a, float b) {
  f32x2 v = {a, b};
  return __builtin_bit_cast(unsigned, __builtin_convertvector(v, bf16x2));
}
DI float geglu(float g, float u) {
  const float c0 = -2.302208198f, c1 = -0.1029432397f;
  float e = __builtin_amdgcn_exp2f(g * __builtin_fmaf(g * g, c1, c0));
  return (g * u) * __builtin_amdgcn_rcpf(1.0f + e);
}
DI unsigned long long next_tag(unsigned long long e) { return e * 6364136223846793005ull + 1442695040888963407ull; }

struct Frag { bf16x8 w[4]; bf16x8 x[2]; };

__global__ __launch_bounds__(512, 2) void geglu_fused(const void* __restrict__ xp, const void* __restrict__ wgp,
                                                      const void* __restrict__ wup, const void* __restrict__ wdp,
                                                      void* __restrict__ outp, void* __restrict__ wsp
) {
#if defined(__HIP_DEVICE_COMPILE__)
  const int tid = threadIdx.x, lane = tid & 63;
  const int wv = __builtin_amdgcn_readfirstlane(tid >> 6);
  const int b = blockIdx.x, xcd = b & 7, r = b >> 3;
  const bool busy = r < 28;
  const int ml = r / 7, fl = r - 7 * ml;
  const int xa = xcd >> 2, xb = xcd & 3;
  const int panel = 4 * xa + ml, fj = 7 * xb + fl;
  const int h = lane >> 5, g1 = (lane >> 4) & 1, qq = (lane >> 2) & 3, p = lane & 3;
  const __amdgpu_buffer_rsrc_t rH = __builtin_amdgcn_make_buffer_rsrc(wsp, 0, FLAG_OFF + FLAG_BYTES, 0x00020000);
  STAMP(0);

  if (wv < 4) {
    if (!busy) return;
    const int rb = 4 * panel + wv;
    const unsigned fo = FLAG_OFF + (((rb * 8 + (lane & 7)) * 4 + xb) << 7);
    u32x2 ep = __builtin_amdgcn_raw_buffer_load_b64(rH, fo + 56, 0, 16);

    unsigned ax[4], aw[4];
    {
      const unsigned a0 = 16384 + wv * 8192 + (lane & 31) * 128 + ((h ^ ((lane >> 1) & 7)) << 4);
      const int m = p >> 1;
      const unsigned w0 = m * 8192 + (8 * h + qq) * 128 + ((g1 ^ (4 * m + 2 * (qq >> 1))) << 4) + (p & 1) * 8;
#pragma unroll
      for (int i = 0; i < 4; ++i) { ax[i] = a0 ^ (i << 5); aw[i] = w0 ^ (i << 5); }
    }
    f32x16 acc[2][4];
#pragma unroll
    for (int i = 0; i < 2; ++i)
#pragma unroll
      for (int j = 0; j < 4; ++j)
#pragma unroll
        for (int e = 0; e < 16; ++e) acc[i][j][e] = 0.f;

#define LOADF(Fv, so, s)                                                                   \
  do {                                                                                     \
    _Pragma("unroll") for (int mt = 0; mt < 2; ++mt)                                       \
        Fv.x[mt] = *(const LAS bf16x8*)(ax[s] + (so) + mt * 4096);                         \
    _Pragma("unroll") for (int t = 0; t < 4; ++t)                                          \
        Fv.w[t] = cat(trd(aw[t] + (so) + (s) * 2048), trd(aw[t] + (so) + (s) * 2048 + 512)); \
  } while (0)
#define MMA(Fv)                                                                            \
  do {                                                                                     \
    _Pragma("unroll") for (int t = 0; t < 4; ++t)                                          \
        _Pragma("unroll") for (int mt = 0; mt < 2; ++mt)                                   \
            acc[mt][t] = MFMA(Fv.w[t], Fv.x[mt], acc[mt][t]);                              \
  } while (0)
#define ILV()                                                                              \
  do {                                                                                     \
    _Pragma("unroll") for (int i = 0; i < 5; ++i) {                                        \
      __builtin_amdgcn_sched_group_barrier(0x8, 1, 0);                                     \
      __builtin_amdgcn_sched_group_barrier(0x100, 2, 0);                                   \
    }                                                                                      \
    __builtin_amdgcn_sched_group_barrier(0x8, 3, 0);                                       \
    __builtin_amdgcn_sched_barrier(0);                                                     \
  } while (0)

    Frag f0, f1;
#pragma unroll
    for (int j = 0; j < 4; ++j) asm volatile("" : "+v"(acc[0][j]), "+v"(acc[1][j]), "+v"(ax[j]), "+v"(aw[j]));
    BAR();
    STAMP(1);
    unsigned so = 0;
    LOADF(f0, so, 0);
    __builtin_amdgcn_sched_barrier(0);
#pragma unroll 1
    for (int s = 0; s < 8; ++s) {
      LOADF(f1, so, 1); MMA(f0); ILV();
      LOADF(f0, so, 2); MMA(f1); ILV();
      LOADF(f1, so, 3); MMA(f0); ILV();
      WAITL0();
      BAR();
      __builtin_amdgcn_sched_barrier(0);
      so = (so == 2 * SLOT) ? 0u : so + SLOT;
      LOADF(f0, so, 0);
      MMA(f1); ILV();
    }
    STAMP(2);
    const unsigned ho = (unsigned)(rb * 28 + fj) * 8192 + lane * 16;
#pragma unroll
    for (int t = 0; t < 4; ++t)
#pragma unroll
      for (int mt = 0; mt < 2; ++mt) {
        const f32x16 a = acc[mt][t];
        u32x4 o;
        o[0] = pk(geglu(a[0], a[4]), geglu(a[1], a[5]));
        o[1] = pk(geglu(a[2], a[6]), geglu(a[3], a[7]));
        o[2] = pk(geglu(a[8], a[12]), geglu(a[9], a[13]));
        o[3] = pk(geglu(a[10], a[14]), geglu(a[11], a[15]));
        __builtin_amdgcn_raw_buffer_store_b128(o, rH, ho + (t * 2 + mt) * 1024, 0, 16);
      }
    STAMP(3);
    const unsigned long long tag = next_tag(((unsigned long long)ep[1] << 32) | ep[0]);
    WAITV(0);
    if (lane < 8) {
      u32x2 tg = {(unsigned)tag, (unsigned)(tag >> 32)};
      __builtin_amdgcn_raw_buffer_store_b64(tg, rH, fo + fl * 8, 0, 16);
    }
    STAMP(4);
    return;
  }

  const int q = wv - 4;
  const int rb = 4 * xcd + (r >> 3), dj = r & 7;
  const u32x4 sD = mksrd(wdp, F * D * 2);
  const unsigned vd = (lane >> 3) * 1024 + (((lane & 7) ^ (4 * ((lane >> 4) & 1))) << 4);
  const unsigned dso = (unsigned)q * 448 * 1024 + dj * 128;
  const unsigned lsp = SPARE + q * 4096, lq = q * 12288;
  if (busy) {
    const u32x4 sX = mksrd(xp, S * D * 2);
    const u32x4 sW = mksrd(q < 2 ? wgp : wup, F * D * 2);
    const unsigned vx0 = (lane >> 3) * 1024 + (((lane & 7) ^ (lane >> 4)) << 4), vx1 = vx0 ^ 64;
    const unsigned vw = (lane >> 3) * 3584 + (((lane & 7) ^ (4 * (q >> 1) + 2 * ((lane >> 4) & 1))) << 4);
    const unsigned xso = (unsigned)(panel * 256 + q * 64) * 1024;
    const unsigned wso = (unsigned)(q & 1) * 32 * 3584 + fj * 128;
    int ks = (((xb + 2 * xa) & 3) + 4 * ((ml + fl) & 1)) & 7;
    unsigned slot = 0;
#define ISSUE()                                                                   \
  do {                                                                            \
    dma_run<4, 8 * 3584>(slot + q * 4096, vw, vw, sW, wso + ks * 64 * 3584);      \
    dma_run<8, 8192>(slot + 16384 + q * 8192, vx0, vx1, sX, xso + ks * 128);      \
    ks = (ks + 1) & 7;                                                            \
    slot = (slot == 2 * SLOT) ? 0u : slot + SLOT;                                 \
  } while (0)
    ISSUE(); ISSUE(); ISSUE();
    WAITV(24);
    BAR();
#pragma unroll 1
    for (int s = 1; s <= 5; ++s) {
      WAITV(12);
      BAR();
      ISSUE();
    }
    dma_run<4, 8192>(lsp, vd, vd, sD, dso);
    WAITV(16);
    BAR();
    dma_run<12, 8192>(2 * SLOT + lq, vd, vd, sD, dso + 4 * 8192);
    WAITV(16);
    BAR();
    dma_run<12, 8192>(lq, vd, vd, sD, dso + 16 * 8192);
    BAR();
  } else {
    dma_run<4, 8192>(lsp, vd, vd, sD, dso);
    dma_run<12, 8192>(2 * SLOT + lq, vd, vd, sD, dso + 4 * 8192);
    dma_run<12, 8192>(lq, vd, vd, sD, dso + 16 * 8192);
  }
  dma_run<12, 8192>(SLOT + lq, vd, vd, sD, dso + 28 * 8192);
  STAMP(1);
  const unsigned d0 = (4 * h + qq) * 128 + (((2 * g1 + (p >> 1)) ^ (4 * (qq >> 1))) << 4) + (p & 1) * 8;
  const unsigned aq[2] = {lq + d0, lq + (d0 ^ 64)};
  const unsigned as[2] = {lsp + d0, lsp + (d0 ^ 64)};
  bf16x8 wr[8][2];
  WAITV(24);
#pragma unroll
  for (int k = 0; k < 8; ++k)
#pragma unroll
    for (int dt = 0; dt < 2; ++dt) {
      const unsigned a = k < 2 ? as[dt] + k * 2048 : aq[dt] + 2 * SLOT + (k - 2) * 2048;
      wr[k][dt] = cat(trd(a), trd(a + 1024));
    }
#pragma unroll
  for (int k = 0; k < 8; ++k) asm volatile("" : "+v"(wr[k][0]), "+v"(wr[k][1]));
  WAITL0();
  dma_run<4, 8192>(lsp, vd, vd, sD, dso + 40 * 8192);
  dma_run<12, 8192>(2 * SLOT + lq, vd, vd, sD, dso + 44 * 8192);
  STAMP(2);

  {
    const unsigned fo = FLAG_OFF + (((rb * 8 + dj) * 4 + q) << 7) + (lane & 7) * 8;
    unsigned long long tag = 0;
    for (int it = 0; it < (1 << 20); ++it) {
      asm volatile("" ::: "memory");
      const u32x2 v = __builtin_amdgcn_raw_buffer_load_b64(rH, fo, 0, 16);
      const unsigned elo = __builtin_amdgcn_readlane(v[0], 7), ehi = __builtin_amdgcn_readlane(v[1], 7);
      tag = next_tag(((unsigned long long)ehi << 32) | elo);
      const bool ok = (((unsigned long long)v[1] << 32) | v[0]) == tag;
      if ((__builtin_amdgcn_ballot_w64(ok) & 0x7f) == 0x7f) break;
    }
    if (lane == 7) {
      u32x2 tg = {(unsigned)tag, (unsigned)(tag >> 32)};
      __builtin_amdgcn_raw_buffer_store_b64(tg, rH, fo, 0, 16);
    }
    asm volatile("" ::: "memory");
  }
  STAMP(3);
  f32x16 acc[2][2];
#pragma unroll
  for (int i = 0; i < 2; ++i)
#pragma unroll
    for (int j = 0; j < 2; ++j)
#pragma unroll
      for (int e = 0; e < 16; ++e) acc[i][j][e] = 0.f;
  const unsigned ho = (unsigned)(rb * 28 + 7 * q) * 8192 + lane * 16;
  constexpr int WIN = 8;
  u32x4 hf[WIN][2];
#pragma unroll
  for (int k = 0; k < WIN; ++k)
#pragma unroll
    for (int mt = 0; mt < 2; ++mt) hf[k][mt] = __builtin_amdgcn_raw_buffer_load_b128(rH, ho, (k * 2 + mt) * 1024, 16);
  __builtin_amdgcn_sched_barrier(0);
  const unsigned a2[2] = {aq[0] + 2 * SLOT, aq[1] + 2 * SLOT};
  bf16x8 an[2] = {wr[0][0], wr[0][1]};
#pragma unroll
  for (int k = 0; k < 28; ++k) {
    const bf16x8 a[2] = {an[0], an[1]};
    const int kn = k + 1;
#pragma unroll
    for (int dt = 0; dt < 2; ++dt) {
      if (kn < 8) an[dt] = wr[kn][dt];
      else if (kn < 28) {
        const unsigned ad = kn < 14 ? aq[dt] + (kn - 8) * 2048
                          : kn < 20 ? aq[dt] + SLOT + (kn - 14) * 2048
                          : kn < 22 ? as[dt] + (kn - 20) * 2048
                                    : a2[dt] + (kn - 22) * 2048;
        an[dt] = cat(trd(ad), trd(ad + 1024));
      }
    }
    __builtin_amdgcn_sched_barrier(0);
#pragma unroll
    for (int mt = 0; mt < 2; ++mt) {
      const bf16x8 hb = __builtin_bit_cast(bf16x8, hf[k % WIN][mt]);
#pragma unroll
      for (int dt = 0; dt < 2; ++dt) acc[mt][dt] = MFMA(a[dt], hb, acc[mt][dt]);
      if (k + WIN < 28) hf[k % WIN][mt] = __builtin_amdgcn_raw_buffer_load_b128(rH, ho, ((k + WIN) * 2 + mt) * 1024, 16);
      __builtin_amdgcn_sched_barrier(0);
    }
  }
  STAMP(4);
  WAITL0();
#pragma unroll
  for (int t = 0; t < 4; ++t) {
    if (t != q) {
      const int pos = t < q ? t : t - 1;
#pragma unroll
      for (int g = 0; g < 4; ++g) {
        const f32x16 a = acc[t >> 1][t & 1];
        f32x4 v = {a[4 * g], a[4 * g + 1], a[4 * g + 2], a[4 * g + 3]};
        *(LAS f32x4*)(lq + pos * 4096 + g * 1024 + lane * 16) = v;
      }
    }
  }
  f32x16 mine = q == 0 ? acc[0][0] : q == 1 ? acc[0][1] : q == 2 ? acc[1][0] : acc[1][1];
  WAITL0();
  BAR();
#pragma unroll
  for (int o = 1; o < 4; ++o) {
    const int src = (q + o) & 3;
    const int pos = q < src ? q : q - 1;
#pragma unroll
    for (int g = 0; g < 4; ++g) {
      const f32x4 v = *(const LAS f32x4*)(src * 12288 + pos * 4096 + g * 1024 + lane * 16);
#pragma unroll
      for (int e = 0; e < 4; ++e) mine[4 * g + e] += v[e];
    }
  }
  {
    const __amdgpu_buffer_rsrc_t rO = __builtin_amdgcn_make_buffer_rsrc(outp, 0, S * D * 2, 0x00020000);
    const unsigned oo = (unsigned)(rb * 64 + (q >> 1) * 32 + (lane & 31)) * 1024 + dj * 128 + (q & 1) * 64 + h * 16;
#pragma unroll
    for (int gp = 0; gp < 2; ++gp) {
      unsigned a0 = pk(mine[8 * gp], mine[8 * gp + 1]), a1 = pk(mine[8 * gp + 2], mine[8 * gp + 3]);
      unsigned b0 = pk(mine[8 * gp + 4], mine[8 * gp + 5]), b1 = pk(mine[8 * gp + 6], mine[8 * gp + 7]);
      auto r0 = __builtin_amdgcn_permlane32_swap(a0, b0, false, false);
      auto r1 = __builtin_amdgcn_permlane32_swap(a1, b1, false, false);
      u32x4 o = {r0[0], r1[0], r0[1], r1[1]};
      __builtin_amdgcn_raw_buffer_store_b128(o, rO, oo + gp * 32, 0, 0);
    }
  }
  STAMP(5);
#endif
}

extern "C" void kernel_launch(void* const* d_in, const int* in_sizes, int n_in, void* d_out, int out_size, void* d_ws,
                              size_t ws_size, hipStream_t stream) {
  (void)hipFuncSetAttribute((const void*)geglu_fused, hipFuncAttributeMaxDynamicSharedMemorySize, LDS_BYTES);
  geglu_fused<<<256, 512, LDS_BYTES, stream>>>(d_in[0], d_in[1], d_in[2], d_in[3], d_out, d_ws
  );
}
```

```cpp
#include <hip/hip_runtime.h>
#include <hip/hip_bf16.h>

constexpr int LDS_BYTES = 160 * 1024;

__global__ __launch_bounds__(512, 2) void geglu_fused(const void* x, const void* wg, const void* wu, const void* wd, void* out, void* ws) {
#if defined(__HIP_DEVICE_COMPILE__)
  unsigned tid = threadIdx.x, bid = blockIdx.x;
  auto karg = __builtin_amdgcn_kernarg_segment_ptr();
  asm volatile(
      ".LSTART_%=:\n"
      "s_mov_b32 s14, s2\n"
      "s_load_dwordx2 s[2:3], s[0:1], 0x0\n"
      "s_load_dwordx8 s[4:11], s[0:1], 0x8\n"
      "s_load_dwordx2 s[12:13], s[0:1], 0x28\n"
      "v_readfirstlane_b32 s17, v0\n"
      "v_and_b32 v1, 63, v0\n"
      "s_lshr_b32 s19, s14, 3\n"
      "s_and_b32 s58, s14, 3\n"
      "s_mul_i32 s34, s19, 37\n"
      "s_and_b32 s59, s14, 4\n"
      "s_lshr_b32 s34, s34, 8\n"
      "s_lshr_b32 s17, s17, 6\n"
      "s_and_b32 s55, s17, 3\n"
      "s_cmp_gt_u32 s19, 27\n"
      "s_cbranch_scc0 .LBUSY_%=\n"
      "s_and_b32 s44, s19, 3\n"
      "s_cmp_gt_u32 s17, 3\n"
      "s_cbranch_scc1 .LC_IDLE_%=\n"
      "v_lshrrev_b32 v30, 3, v1\n"
      "v_and_b32 v31, 7, v1\n"
      "s_movk_i32 s37, 0x80\n"
      "s_mov_b32 s38, 7\n"
      "s_mov_b32 s22, 0x1c0000\n"
      "s_cmp_eq_u32 s55, 0\n"
      "s_cbranch_scc1 .LPF_X_%=\n"
      "s_cmp_eq_u32 s55, 3\n"
      "s_cbranch_scc1 .LPF_D_%=\n"
      "s_lshl1_add_u32 s42, s59, 8\n"
      "s_movk_i32 s36, 0xe00\n"
      "s_mov_b32 s38, 6\n"
      "s_lshl1_add_u32 s39, s44, s55\n"
      "s_add_u32 s39, s39, 7\n"
      "s_lshl_b32 s39, s39, 5\n"
      "s_add_u32 s39, s39, s58\n"
      "s_mulk_i32 s39, 896\n"
      "s_mov_b32 s41, 0x38000\n"
      "s_branch .LPF_GO_%=\n"
      ".LPF_X_%=:\n"
      "s_mov_b32 s42, 0\n"
      "s_movk_i32 s36, 0x2000\n"
      "s_movk_i32 s37, 0x400\n"
      "s_add_u32 s39, s59, s44\n"
      "s_lshl2_add_u32 s39, s39, s58\n"
      "s_lshl_b32 s39, s39, 16\n"
      "s_movk_i32 s41, 0x80\n"
      "s_add_u32 s39, s39, s41\n"
      "s_mov_b32 s22, 0x200000\n"
      "s_branch .LPF_GO_%=\n"
      ".LPF_D_%=:\n"
      "s_mov_b32 s42, 24\n"
      "s_movk_i32 s36, 0x400\n"
      "s_and_b32 s39, s14, 7\n"
      "s_lshl3_add_u32 s39, s39, s44\n"
      "s_lshl_b32 s39, s39, 13\n"
      "s_mov_b32 s41, 0x80000\n"
      ".LPF_GO_%=:\n"
      "s_load_dwordx2 s[20:21], s[0:1], s42\n"
      "s_mov_b32 s23, 0x20000\n"
      "v_min_u32 v2, s38, v31\n"
      "v_mul_u32_u24 v2, s37, v2\n"
      "v_mad_u32_u24 v2, v30, s36, v2\n"
      "s_mov_b32 s54, 6\n"
      "s_waitcnt lgkmcnt(0)\n"
      ".LPF_LOOP_%=:\n"
      "buffer_load_dword v4, v2, s[20:23], s39 offen\n"
      "s_add_u32 s39, s39, s41\n"
      "s_cmp_ge_u32 s39, s22\n"
      "s_cselect_b32 s42, 0x1b8000, 0\n"
      "s_sub_u32 s39, s39, s42\n"
      "s_sub_u32 s54, s54, 1\n"
      "s_cbranch_scc0 .LPF_LOOP_%=\n"
      "s_branch .LL_IDLE_%=\n"
      ".LC_IDLE_%=:\n"
      "s_getpc_b64 s[20:21]\n"
      ".LPCREF_%=:\n"
      "s_sub_u32 s20, s20, .LPCREF_%=-.LSTART_%=\n"
      "s_subb_u32 s21, s21, 0\n"
      "s_mov_b32 s22, .LEND_%=-.LSTART_%=\n"
      "s_mov_b32 s23, 0x20000\n"
      "s_and_b32 s42, s19, 3\n"
      "s_lshl_b32 s42, s42, 7\n"
      "s_lshl_b32 s43, s55, 15\n"
      "s_add_u32 s42, s42, s43\n"
      "v_lshlrev_b32 v2, 9, v1\n"
      "buffer_load_dword v4, v2, s[20:23], s42 offen\n"
      "s_cmp_eq_u32 s19, 28\n"
      "s_cbranch_scc1 .LTLB_%=\n"
      "s_endpgm\n"
      ".LBUSY_%=:\n"
      "s_cmp_gt_u32 s17, 3\n"
      "s_cbranch_scc1 .LC_BUSY_%=\n"
      "v_lshrrev_b32 v30, 3, v1\n"
      "v_and_b32 v31, 7, v1\n"
      "s_mov_b32 s40, 0\n"
      "s_mov_b32 s15, s55\n"
      "v_lshrrev_b32 v2, 4, v1\n"
      "s_add_u32 s59, s59, s34\n"
      "v_xor_b32 v2, v2, v31\n"
      "s_lshl2_add_u32 s42, s59, s15\n"
      "v_lshlrev_b32 v2, 4, v2\n"
      "s_lshl2_add_u32 s42, s42, s58\n"
      "v_lshl_or_b32 v2, v30, 11, v2\n"
      "s_lshl_b32 s36, s42, 14\n"
      "v_xor_b32 v3, 64, v2\n"
      "s_lshl_b32 s46, s15, 13\n"
      "s_add_u32 s46, s46, 0x10000\n"
      "s_mul_i32 s42, s34, 7\n"
      "s_sub_u32 s35, s19, s42\n"
      "s_mul_i32 s67, s58, 7\n"
      "s_add_u32 s67, s67, s35\n"
      "s_lshl_b32 s16, s15, 12\n"
      "v_bfe_u32 v6, v1, 4, 1\n"
      "v_lshlrev_b32 v6, 1, v6\n"
      "v_xor_b32 v6, v6, v31\n"
      "v_mul_u32_u24 v8, 0xe00, v30\n"
      "v_lshl_or_b32 v6, v6, 4, v8\n"
      "v_xor_b32 v7, 64, v6\n"
      "v_add_u32 v7, 0x6c00, v7\n"
      "v_add_u32 v8, 0xd800, v6\n"
      "v_add_u32 v9, 0xd800, v7\n"
      "s_and_b32 s42, s15, 1\n"
      "s_mul_i32 s41, s42, 0x1c000\n"
      "s_lshl_b32 s42, s67, 7\n"
      "s_add_u32 s41, s41, s42\n"
      "s_lshl_b32 s42, s40, 7\n"
      "v_or_b32 v4, s42, v2\n"
      "v_or_b32 v5, s42, v3\n"
      "s_xor_b32 s37, s36, 0x4000\n"
      "s_xor_b32 s38, s36, 0x8000\n"
      "s_xor_b32 s39, s36, 0xc000\n"
      "s_mov_b32 s22, 0x200000\n"
      "s_mov_b32 s23, 0x20000\n"
      "s_mov_b32 s26, 0x1c0000\n"
      "s_mov_b32 s27, 0x20000\n"
      "s_cmp_lt_u32 s15, 2\n"
      "s_waitcnt lgkmcnt(0)\n"
      "s_mov_b64 s[20:21], s[2:3]\n"
      "s_cselect_b64 s[24:25], s[4:5], s[6:7]\n"
      "s_bitcmp1_b32 s19, 0\n"
      "s_cbranch_scc1 .LORD_B_%=\n"
      "s_mov_b32 m0, s46\n"
      "s_nop 0\n"
      "buffer_load_dwordx4 v4, s[20:23], s36 offen lds\n"
      "buffer_load_dwordx4 v5, s[20:23], s36 offen offset:1024 lds\n"
      "s_addk_i32 m0, 0x800\n"
      "s_nop 0\n"
      "buffer_load_dwordx4 v4, s[20:23], s37 offen lds\n"
      "buffer_load_dwordx4 v5, s[20:23], s37 offen offset:1024 lds\n"
      "s_addk_i32 m0, 0x800\n"
      "s_nop 0\n"
      "buffer_load_dwordx4 v4, s[20:23], s38 offen lds\n"
      "buffer_load_dwordx4 v5, s[20:23], s38 offen offset:1024 lds\n"
      "s_addk_i32 m0, 0x800\n"
      "s_nop 0\n"
      "buffer_load_dwordx4 v4, s[20:23], s39 offen lds\n"
      "buffer_load_dwordx4 v5, s[20:23], s39 offen offset:1024 lds\n"
      "s_mov_b32 m0, s16\n"
      "s_mul_i32 s43, s40, 0x38000\n"
      "s_add_u32 s43, s43, s41\n"
      "buffer_load_dwordx4 v6, s[24:27], s43 offen lds\n"
      "buffer_load_dwordx4 v7, s[24:27], s43 offen offset:1024 lds\n"
      "buffer_load_dwordx4 v8, s[24:27], s43 offen offset:2048 lds\n"
      "buffer_load_dwordx4 v9, s[24:27], s43 offen offset:3072 lds\n"
      "s_branch .LORD_E_%=\n"
      ".LORD_B_%=:\n"
      "s_mov_b32 m0, s16\n"
      "s_mul_i32 s43, s40, 0x38000\n"
      "s_add_u32 s43, s43, s41\n"
      "buffer_load_dwordx4 v6, s[24:27], s43 offen lds\n"
      "buffer_load_dwordx4 v7, s[24:27], s43 offen offset:1024 lds\n"
      "buffer_load_dwordx4 v8, s[24:27], s43 offen offset:2048 lds\n"
      "buffer_load_dwordx4 v9, s[24:27], s43 offen offset:3072 lds\n"
      "s_add_u32 m0, s46, 0x1800\n"
      "s_nop 0\n"
      "buffer_load_dwordx4 v4, s[20:23], s39 offen lds\n"
      "buffer_load_dwordx4 v5, s[20:23], s39 offen offset:1024 lds\n"
      "s_add_u32 m0, s46, 0x1000\n"
      "s_nop 0\n"
      "buffer_load_dwordx4 v4, s[20:23], s38 offen lds\n"
      "buffer_load_dwordx4 v5, s[20:23], s38 offen offset:1024 lds\n"
      "s_mov_b32 m0, s46\n"
      "s_nop 0\n"
      "buffer_load_dwordx4 v4, s[20:23], s36 offen lds\n"
      "buffer_load_dwordx4 v5, s[20:23], s36 offen offset:1024 lds\n"
      "s_addk_i32 m0, 0x800\n"
      "s_nop 0\n"
      "buffer_load_dwordx4 v4, s[20:23], s37 offen lds\n"
      "buffer_load_dwordx4 v5, s[20:23], s37 offen offset:1024 lds\n"
      ".LORD_E_%=:\n"
      "s_lshl_b32 s47, s55, 12\n"
      "s_mov_b64 s[28:29], s[8:9]\n"
      "s_mov_b32 s30, 0x1c0000\n"
      "s_mov_b32 s31, 0x20000\n"
      "s_mov_b64 s[68:69], s[10:11]\n"
      "s_mov_b32 s70, 0x200000\n"
      "s_mov_b32 s71, 0x20000\n"
      "s_mov_b64 s[76:77], s[12:13]\n"
      "s_mov_b32 s78, 0x820000\n"
      "s_mov_b32 s79, 0x20000\n"
      "s_and_b32 s18, s14, 7\n"
      "s_lshr_b32 s44, s19, 3\n"
      "s_lshl2_add_u32 s50, s18, s44\n"
      "s_and_b32 s51, s19, 7\n"
      "s_mul_i32 s48, s55, 0x70000\n"
      "s_lshl_b32 s42, s51, 7\n"
      "s_add_u32 s48, s48, s42\n"
      "s_lshl2_add_u32 s42, s50, s55\n"
      "s_mul_i32 s49, s42, 0xe000\n"
      "s_and_b32 s42, s51, 3\n"
      "s_mul_i32 s42, s42, 2\n"
      "s_and_b32 s44, s44, 1\n"
      "s_mul_i32 s43, s44, 3\n"
      "s_add_u32 s60, s42, s43\n"
      "s_mul_i32 s42, s60, 37\n"
      "s_lshr_b32 s42, s42, 8\n"
      "s_mul_i32 s42, s42, 7\n"
      "s_sub_u32 s60, s60, s42\n"
      "s_add_u32 s61, s60, 1\n"
      "s_cmp_eq_u32 s61, 7\n"
      "s_cselect_b32 s61, 0, s61\n"
      "s_add_u32 s62, s61, 1\n"
      "s_cmp_eq_u32 s62, 7\n"
      "s_cselect_b32 s62, 0, s62\n"
      "s_add_u32 s63, s62, 1\n"
      "s_cmp_eq_u32 s63, 7\n"
      "s_cselect_b32 s63, 0, s63\n"
      "s_add_u32 s64, s63, 1\n"
      "s_cmp_eq_u32 s64, 7\n"
      "s_cselect_b32 s64, 0, s64\n"
      "s_add_u32 s65, s64, 1\n"
      "s_cmp_eq_u32 s65, 7\n"
      "s_cselect_b32 s65, 0, s65\n"
      "s_add_u32 s66, s65, 1\n"
      "s_cmp_eq_u32 s66, 7\n"
      "s_cselect_b32 s66, 0, s66\n"
      "v_lshlrev_b32 v10, 4, v31\n"
      "v_lshl_or_b32 v10, v30, 12, v10\n"
      "v_xor_b32 v11, 32, v10\n"
      "v_xor_b32 v12, 64, v10\n"
      "v_xor_b32 v13, 96, v10\n"
      "s_lshl3_add_u32 s42, s50, s51\n"
      "s_lshl2_add_u32 s42, s42, s55\n"
      "s_lshl_b32 s42, s42, 7\n"
      "s_add_u32 s42, s42, 0x800000\n"
      "v_mov_b32 v14, s42\n"
      "s_add_u32 s74, s12, s42\n"
      "s_addc_u32 s75, s13, 0\n"
      "v_lshlrev_b32 v15, 4, v1\n"
      "v_bfe_u32 v27, v1, 2, 2\n"
      "v_lshrrev_b32 v29, 4, v1\n"
      "v_lshl_add_u32 v29, v27, 3, v29\n"
      "v_lshlrev_b32 v29, 7, v29\n"
      "v_lshl_or_b32 v29, v27, 5, v29\n"
      "v_and_b32 v27, 3, v1\n"
      "v_lshl_or_b32 v29, v27, 3, v29\n"
      "v_add_u32 v16, s47, v29\n"
      "v_xor_b32 v17, 32, v16\n"
      "v_xor_b32 v18, 64, v16\n"
      "v_xor_b32 v19, 96, v16\n"
      "v_add_u32 v20, 0x10000, v16\n"
      "v_add_u32 v21, 0x10000, v17\n"
      "v_add_u32 v22, 0x10000, v18\n"
      "v_add_u32 v23, 0x10000, v19\n"
      "v_add_u32 v24, 0x20000, v16\n"
      "v_add_u32 v25, 0x20000, v17\n"
      "v_add_u32 v26, 0x20000, v18\n"
      "v_add_u32 v27, 0x20000, v19\n"
      "s_add_u32 s40, s40, 1\n"
      "s_and_b32 s40, s40, 7\n"
      "s_lshl_b32 s42, s40, 7\n"
      "v_or_b32 v4, s42, v2\n"
      "v_or_b32 v5, s42, v3\n"
      "s_add_u32 m0, s46, 0x8000\n"
      "s_nop 0\n"
      "buffer_load_dwordx4 v4, s[20:23], s36 offen lds\n"
      "buffer_load_dwordx4 v5, s[20:23], s36 offen offset:1024 lds\n"
      "s_addk_i32 m0, 0x800\n"
      "s_nop 0\n"
      "buffer_load_dwordx4 v4, s[20:23], s37 offen lds\n"
      "buffer_load_dwordx4 v5, s[20:23], s37 offen offset:1024 lds\n"
      "s_addk_i32 m0, 0x800\n"
      "s_nop 0\n"
      "buffer_load_dwordx4 v4, s[20:23], s38 offen lds\n"
      "buffer_load_dwordx4 v5, s[20:23], s38 offen offset:1024 lds\n"
      "s_addk_i32 m0, 0x800\n"
      "s_nop 0\n"
      "buffer_load_dwordx4 v4, s[20:23], s39 offen lds\n"
      "buffer_load_dwordx4 v5, s[20:23], s39 offen offset:1024 lds\n"
      "s_add_u32 m0, s16, 0x4000\n"
      "s_mul_i32 s43, s40, 0x38000\n"
      "s_add_u32 s43, s43, s41\n"
      "s_waitcnt vmcnt(8)\n"
      "s_barrier\n"
      "buffer_load_dwordx4 v6, s[24:27], s43 offen lds\n"
      "buffer_load_dwordx4 v7, s[24:27], s43 offen offset:1024 lds\n"
      "buffer_load_dwordx4 v8, s[24:27], s43 offen offset:2048 lds\n"
      "buffer_load_dwordx4 v9, s[24:27], s43 offen offset:3072 lds\n"
      "s_add_u32 s40, s40, 1\n"
      "s_and_b32 s40, s40, 7\n"
      "s_lshl_b32 s42, s40, 7\n"
      "v_or_b32 v4, s42, v2\n"
      "v_or_b32 v5, s42, v3\n"
      "s_add_u32 m0, s46, 0x10000\n"
      "s_nop 0\n"
      "buffer_load_dwordx4 v4, s[20:23], s36 offen lds\n"
      "buffer_load_dwordx4 v5, s[20:23], s36 offen offset:1024 lds\n"
      "s_addk_i32 m0, 0x800\n"
      "s_nop 0\n"
      "buffer_load_dwordx4 v4, s[20:23], s37 offen lds\n"
      "buffer_load_dwordx4 v5, s[20:23], s37 offen offset:1024 lds\n"
      "s_addk_i32 m0, 0x800\n"
      "s_nop 0\n"
      "buffer_load_dwordx4 v4, s[20:23], s38 offen lds\n"
      "buffer_load_dwordx4 v5, s[20:23], s38 offen offset:1024 lds\n"
      "s_addk_i32 m0, 0x800\n"
      "s_nop 0\n"
      "buffer_load_dwordx4 v4, s[20:23], s39 offen lds\n"
      "buffer_load_dwordx4 v5, s[20:23], s39 offen offset:1024 lds\n"
      "s_mov_b32 m0, s16\n"
      "s_mul_i32 s43, s40, 0x38000\n"
      "s_add_u32 s43, s43, s41\n"
      "s_waitcnt vmcnt(8)\n"
      "s_barrier\n"
      "buffer_load_dwordx4 v6, s[24:27], s43 offen lds\n"
      "buffer_load_dwordx4 v7, s[24:27], s43 offen offset:1024 lds\n"
      "buffer_load_dwordx4 v8, s[24:27], s43 offen offset:2048 lds\n"
      "buffer_load_dwordx4 v9, s[24:27], s43 offen offset:3072 lds\n"
      "s_add_u32 s40, s40, 1\n"
      "s_and_b32 s40, s40, 7\n"
      "s_lshl_b32 s42, s40, 7\n"
      "v_or_b32 v4, s42, v2\n"
      "v_or_b32 v5, s42, v3\n"
      "s_mov_b32 m0, s46\n"
      "s_nop 0\n"
      "buffer_load_dwordx4 v4, s[20:23], s36 offen lds\n"
      "buffer_load_dwordx4 v5, s[20:23], s36 offen offset:1024 lds\n"
      "s_addk_i32 m0, 0x800\n"
      "s_nop 0\n"
      "buffer_load_dwordx4 v4, s[20:23], s37 offen lds\n"
      "buffer_load_dwordx4 v5, s[20:23], s37 offen offset:1024 lds\n"
      "s_addk_i32 m0, 0x800\n"
      "s_nop 0\n"
      "buffer_load_dwordx4 v4, s[20:23], s38 offen lds\n"
      "buffer_load_dwordx4 v5, s[20:23], s38 offen offset:1024 lds\n"
      "s_addk_i32 m0, 0x800\n"
      "s_nop 0\n"
      "buffer_load_dwordx4 v4, s[20:23], s39 offen lds\n"
      "buffer_load_dwordx4 v5, s[20:23], s39 offen offset:1024 lds\n"
      "s_add_u32 m0, s47, 0x8000\n"
      "s_lshl_b32 s43, s60, 16\n"
      "s_add_u32 s43, s43, s48\n"
      "s_waitcnt vmcnt(8)\n"
      "s_barrier\n"
      "buffer_load_dwordx4 v10, s[28:31], s43 offen lds\n"
      "buffer_load_dwordx4 v11, s[28:31], s43 offen offset:1024 lds\n"
      "buffer_load_dwordx4 v12, s[28:31], s43 offen offset:2048 lds\n"
      "buffer_load_dwordx4 v13, s[28:31], s43 offen offset:3072 lds\n"
      "s_add_u32 m0, s16, 0x4000\n"
      "s_mul_i32 s43, s40, 0x38000\n"
      "s_add_u32 s43, s43, s41\n"
      "buffer_load_dwordx4 v6, s[24:27], s43 offen lds\n"
      "buffer_load_dwordx4 v7, s[24:27], s43 offen offset:1024 lds\n"
      "buffer_load_dwordx4 v8, s[24:27], s43 offen offset:2048 lds\n"
      "buffer_load_dwordx4 v9, s[24:27], s43 offen offset:3072 lds\n"
      "s_add_u32 s40, s40, 1\n"
      "s_and_b32 s40, s40, 7\n"
      "s_lshl_b32 s42, s40, 7\n"
      "v_or_b32 v4, s42, v2\n"
      "v_or_b32 v5, s42, v3\n"
      "s_add_u32 m0, s46, 0x8000\n"
      "s_nop 0\n"
      "buffer_load_dwordx4 v4, s[20:23], s36 offen lds\n"
      "buffer_load_dwordx4 v5, s[20:23], s36 offen offset:1024 lds\n"
      "s_addk_i32 m0, 0x800\n"
      "s_nop 0\n"
      "buffer_load_dwordx4 v4, s[20:23], s37 offen lds\n"
      "buffer_load_dwordx4 v5, s[20:23], s37 offen offset:1024 lds\n"
      "s_addk_i32 m0, 0x800\n"
      "s_nop 0\n"
      "buffer_load_dwordx4 v4, s[20:23], s38 offen lds\n"
      "buffer_load_dwordx4 v5, s[20:23], s38 offen offset:1024 lds\n"
      "s_addk_i32 m0, 0x800\n"
      "s_nop 0\n"
      "buffer_load_dwordx4 v4, s[20:23], s39 offen lds\n"
      "buffer_load_dwordx4 v5, s[20:23], s39 offen offset:1024 lds\n"
      "s_add_u32 m0, s47, 0xc000\n"
      "s_lshl_b32 s43, s61, 16\n"
      "s_add_u32 s43, s43, s48\n"
      "s_waitcnt vmcnt(8)\n"
      "s_barrier\n"
      "buffer_load_dwordx4 v10, s[28:31], s43 offen lds\n"
      "buffer_load_dwordx4 v11, s[28:31], s43 offen offset:1024 lds\n"
      "buffer_load_dwordx4 v12, s[28:31], s43 offen offset:2048 lds\n"
      "buffer_load_dwordx4 v13, s[28:31], s43 offen offset:3072 lds\n"
      "s_mov_b32 m0, s16\n"
      "s_mul_i32 s43, s40, 0x38000\n"
      "s_add_u32 s43, s43, s41\n"
      "buffer_load_dwordx4 v6, s[24:27], s43 offen lds\n"
      "buffer_load_dwordx4 v7, s[24:27], s43 offen offset:1024 lds\n"
      "buffer_load_dwordx4 v8, s[24:27], s43 offen offset:2048 lds\n"
      "buffer_load_dwordx4 v9, s[24:27], s43 offen offset:3072 lds\n"
      "ds_read_b64_tr_b16 v[32:33], v16 offset:32768\n"
      "ds_read_b64_tr_b16 v[34:35], v16 offset:33280\n"
      "ds_read_b64_tr_b16 v[36:37], v17 offset:32768\n"
      "ds_read_b64_tr_b16 v[38:39], v17 offset:33280\n"
      "ds_read_b64_tr_b16 v[40:41], v18 offset:32768\n"
      "ds_read_b64_tr_b16 v[42:43], v18 offset:33280\n"
      "ds_read_b64_tr_b16 v[44:45], v19 offset:32768\n"
      "ds_read_b64_tr_b16 v[46:47], v19 offset:33280\n"
      "s_add_u32 s40, s40, 1\n"
      "s_and_b32 s40, s40, 7\n"
      "s_lshl_b32 s42, s40, 7\n"
      "v_or_b32 v4, s42, v2\n"
      "v_or_b32 v5, s42, v3\n"
      "s_add_u32 m0, s46, 0x10000\n"
      "s_nop 0\n"
      "buffer_load_dwordx4 v4, s[20:23], s36 offen lds\n"
      "buffer_load_dwordx4 v5, s[20:23], s36 offen offset:1024 lds\n"
      "s_addk_i32 m0, 0x800\n"
      "s_nop 0\n"
      "buffer_load_dwordx4 v4, s[20:23], s37 offen lds\n"
      "buffer_load_dwordx4 v5, s[20:23], s37 offen offset:1024 lds\n"
      "s_addk_i32 m0, 0x800\n"
      "s_nop 0\n"
      "buffer_load_dwordx4 v4, s[20:23], s38 offen lds\n"
      "buffer_load_dwordx4 v5, s[20:23], s38 offen offset:1024 lds\n"
      "s_addk_i32 m0, 0x800\n"
      "s_nop 0\n"
      "buffer_load_dwordx4 v4, s[20:23], s39 offen lds\n"
      "buffer_load_dwordx4 v5, s[20:23], s39 offen offset:1024 lds\n"
      "s_waitcnt lgkmcnt(0)\n"
      "s_add_u32 m0, s47, 0x8000\n"
      "s_lshl_b32 s43, s62, 16\n"
      "s_add_u32 s43, s43, s48\n"
      "s_waitcnt vmcnt(8)\n"
      "s_barrier\n"
      "buffer_load_dwordx4 v10, s[28:31], s43 offen lds\n"
      "buffer_load_dwordx4 v11, s[28:31], s43 offen offset:1024 lds\n"
      "buffer_load_dwordx4 v12, s[28:31], s43 offen offset:2048 lds\n"
      "buffer_load_dwordx4 v13, s[28:31], s43 offen offset:3072 lds\n"
      "s_add_u32 m0, s16, 0x4000\n"
      "s_mul_i32 s43, s40, 0x38000\n"
      "s_add_u32 s43, s43, s41\n"
      "buffer_load_dwordx4 v6, s[24:27], s43 offen lds\n"
      "buffer_load_dwordx4 v7, s[24:27], s43 offen offset:1024 lds\n"
      "buffer_load_dwordx4 v8, s[24:27], s43 offen offset:2048 lds\n"
      "buffer_load_dwordx4 v9, s[24:27], s43 offen offset:3072 lds\n"
      "ds_read_b64_tr_b16 v[48:49], v16 offset:49152\n"
      "ds_read_b64_tr_b16 v[50:51], v16 offset:49664\n"
      "ds_read_b64_tr_b16 v[52:53], v17 offset:49152\n"
      "ds_read_b64_tr_b16 v[54:55], v17 offset:49664\n"
      "ds_read_b64_tr_b16 v[56:57], v18 offset:49152\n"
      "ds_read_b64_tr_b16 v[58:59], v18 offset:49664\n"
      "ds_read_b64_tr_b16 v[60:61], v19 offset:49152\n"
      "ds_read_b64_tr_b16 v[62:63], v19 offset:49664\n"
      "s_add_u32 s40, s40, 1\n"
      "s_and_b32 s40, s40, 7\n"
      "s_lshl_b32 s42, s40, 7\n"
      "v_or_b32 v4, s42, v2\n"
      "v_or_b32 v5, s42, v3\n"
      "s_mov_b32 m0, s46\n"
      "s_nop 0\n"
      "buffer_load_dwordx4 v4, s[20:23], s36 offen lds\n"
      "buffer_load_dwordx4 v5, s[20:23], s36 offen offset:1024 lds\n"
      "s_addk_i32 m0, 0x800\n"
      "s_nop 0\n"
      "buffer_load_dwordx4 v4, s[20:23], s37 offen lds\n"
      "buffer_load_dwordx4 v5, s[20:23], s37 offen offset:1024 lds\n"
      "s_addk_i32 m0, 0x800\n"
      "s_nop 0\n"
      "buffer_load_dwordx4 v4, s[20:23], s38 offen lds\n"
      "buffer_load_dwordx4 v5, s[20:23], s38 offen offset:1024 lds\n"
      "s_addk_i32 m0, 0x800\n"
      "s_nop 0\n"
      "buffer_load_dwordx4 v4, s[20:23], s39 offen lds\n"
      "buffer_load_dwordx4 v5, s[20:23], s39 offen offset:1024 lds\n"
      "s_waitcnt lgkmcnt(0)\n"
      "s_add_u32 m0, s47, 0xc000\n"
      "s_lshl_b32 s43, s63, 16\n"
      "s_add_u32 s43, s43, s48\n"
      "s_waitcnt vmcnt(8)\n"
      "s_barrier\n"
      "buffer_load_dwordx4 v10, s[28:31], s43 offen lds\n"
      "buffer_load_dwordx4 v11, s[28:31], s43 offen offset:1024 lds\n"
      "buffer_load_dwordx4 v12, s[28:31], s43 offen offset:2048 lds\n"
      "buffer_load_dwordx4 v13, s[28:31], s43 offen offset:3072 lds\n"
      "s_mov_b32 m0, s16\n"
      "s_mul_i32 s43, s40, 0x38000\n"
      "s_add_u32 s43, s43, s41\n"
      "buffer_load_dwordx4 v6, s[24:27], s43 offen lds\n"
      "buffer_load_dwordx4 v7, s[24:27], s43 offen offset:1024 lds\n"
      "buffer_load_dwordx4 v8, s[24:27], s43 offen offset:2048 lds\n"
      "buffer_load_dwordx4 v9, s[24:27], s43 offen offset:3072 lds\n"
      "ds_read_b64_tr_b16 v[64:65], v16 offset:32768\n"
      "ds_read_b64_tr_b16 v[66:67], v16 offset:33280\n"
      "ds_read_b64_tr_b16 v[68:69], v17 offset:32768\n"
      "ds_read_b64_tr_b16 v[70:71], v17 offset:33280\n"
      "ds_read_b64_tr_b16 v[72:73], v18 offset:32768\n"
      "ds_read_b64_tr_b16 v[74:75], v18 offset:33280\n"
      "ds_read_b64_tr_b16 v[76:77], v19 offset:32768\n"
      "ds_read_b64_tr_b16 v[78:79], v19 offset:33280\n"
      "s_add_u32 s40, s40, 1\n"
      "s_and_b32 s40, s40, 7\n"
      "s_lshl_b32 s42, s40, 7\n"
      "v_or_b32 v4, s42, v2\n"
      "v_or_b32 v5, s42, v3\n"
      "s_add_u32 m0, s46, 0x8000\n"
      "s_nop 0\n"
      "buffer_load_dwordx4 v4, s[20:23], s36 offen lds\n"
      "buffer_load_dwordx4 v5, s[20:23], s36 offen offset:1024 lds\n"
      "s_addk_i32 m0, 0x800\n"
      "s_nop 0\n"
      "buffer_load_dwordx4 v4, s[20:23], s37 offen lds\n"
      "buffer_load_dwordx4 v5, s[20:23], s37 offen offset:1024 lds\n"
      "s_addk_i32 m0, 0x800\n"
      "s_nop 0\n"
      "buffer_load_dwordx4 v4, s[20:23], s38 offen lds\n"
      "buffer_load_dwordx4 v5, s[20:23], s38 offen offset:1024 lds\n"
      "s_addk_i32 m0, 0x800\n"
      "s_nop 0\n"
      "buffer_load_dwordx4 v4, s[20:23], s39 offen lds\n"
      "buffer_load_dwordx4 v5, s[20:23], s39 offen offset:1024 lds\n"
      "s_waitcnt lgkmcnt(0)\n"
      "s_add_u32 m0, s16, 0x4000\n"
      "s_mul_i32 s43, s40, 0x38000\n"
      "s_add_u32 s43, s43, s41\n"
      "s_waitcnt vmcnt(8)\n"
      "s_barrier\n"
      "buffer_load_dwordx4 v6, s[24:27], s43 offen lds\n"
      "buffer_load_dwordx4 v7, s[24:27], s43 offen offset:1024 lds\n"
      "buffer_load_dwordx4 v8, s[24:27], s43 offen offset:2048 lds\n"
      "buffer_load_dwordx4 v9, s[24:27], s43 offen offset:3072 lds\n"
      "ds_read_b64_tr_b16 v[80:81], v16 offset:49152\n"
      "ds_read_b64_tr_b16 v[82:83], v16 offset:49664\n"
      "ds_read_b64_tr_b16 v[84:85], v17 offset:49152\n"
      "ds_read_b64_tr_b16 v[86:87], v17 offset:49664\n"
      "ds_read_b64_tr_b16 v[88:89], v18 offset:49152\n"
      "ds_read_b64_tr_b16 v[90:91], v18 offset:49664\n"
      "ds_read_b64_tr_b16 v[92:93], v19 offset:49152\n"
      "ds_read_b64_tr_b16 v[94:95], v19 offset:49664\n"
      "s_add_u32 m0, s47, 0x20000\n"
      "s_lshl_b32 s43, s64, 16\n"
      "s_add_u32 s43, s43, s48\n"
      "buffer_load_dwordx4 v10, s[28:31], s43 offen lds\n"
      "buffer_load_dwordx4 v11, s[28:31], s43 offen offset:1024 lds\n"
      "buffer_load_dwordx4 v12, s[28:31], s43 offen offset:2048 lds\n"
      "buffer_load_dwordx4 v13, s[28:31], s43 offen offset:3072 lds\n"
      "s_add_u32 m0, s47, 0x24000\n"
      "s_lshl_b32 s43, s65, 16\n"
      "s_add_u32 s43, s43, s48\n"
      "buffer_load_dwordx4 v10, s[28:31], s43 offen lds\n"
      "buffer_load_dwordx4 v11, s[28:31], s43 offen offset:1024 lds\n"
      "buffer_load_dwordx4 v12, s[28:31], s43 offen offset:2048 lds\n"
      "buffer_load_dwordx4 v13, s[28:31], s43 offen offset:3072 lds\n"
      "s_waitcnt vmcnt(8)\n"
      "s_barrier\n"
      "s_add_u32 m0, s47, 0x8000\n"
      "s_lshl_b32 s43, s66, 16\n"
      "s_add_u32 s43, s43, s48\n"
      "buffer_load_dwordx4 v10, s[28:31], s43 offen lds\n"
      "buffer_load_dwordx4 v11, s[28:31], s43 offen offset:1024 lds\n"
      "buffer_load_dwordx4 v12, s[28:31], s43 offen offset:2048 lds\n"
      "buffer_load_dwordx4 v13, s[28:31], s43 offen offset:3072 lds\n"
      "s_add_u32 m0, s47, 0x10000\n"
      "s_lshl_b32 s43, s60, 16\n"
      "s_add_u32 s43, s43, s48\n"
      "s_add_u32 s43, s43, 0x8000\n"
      "buffer_load_dwordx4 v10, s[28:31], s43 offen lds\n"
      "buffer_load_dwordx4 v11, s[28:31], s43 offen offset:1024 lds\n"
      "buffer_load_dwordx4 v12, s[28:31], s43 offen offset:2048 lds\n"
      "buffer_load_dwordx4 v13, s[28:31], s43 offen offset:3072 lds\n"
      "s_add_u32 m0, s47, 0x14000\n"
      "s_lshl_b32 s43, s61, 16\n"
      "s_add_u32 s43, s43, s48\n"
      "s_add_u32 s43, s43, 0x8000\n"
      "buffer_load_dwordx4 v10, s[28:31], s43 offen lds\n"
      "buffer_load_dwordx4 v11, s[28:31], s43 offen offset:1024 lds\n"
      "buffer_load_dwordx4 v12, s[28:31], s43 offen offset:2048 lds\n"
      "buffer_load_dwordx4 v13, s[28:31], s43 offen offset:3072 lds\n"
      "s_mov_b32 m0, s47\n"
      "s_lshl_b32 s43, s62, 16\n"
      "s_add_u32 s43, s43, s48\n"
      "s_add_u32 s43, s43, 0x8000\n"
      "buffer_load_dwordx4 v10, s[28:31], s43 offen lds\n"
      "buffer_load_dwordx4 v11, s[28:31], s43 offen offset:1024 lds\n"
      "buffer_load_dwordx4 v12, s[28:31], s43 offen offset:2048 lds\n"
      "buffer_load_dwordx4 v13, s[28:31], s43 offen offset:3072 lds\n"
      "s_waitcnt lgkmcnt(0)\n"
      "s_add_u32 m0, s47, 0xc000\n"
      "s_lshl_b32 s43, s63, 16\n"
      "s_add_u32 s43, s43, s48\n"
      "s_add_u32 s43, s43, 0x8000\n"
      "buffer_load_dwordx4 v10, s[28:31], s43 offen lds\n"
      "buffer_load_dwordx4 v11, s[28:31], s43 offen offset:1024 lds\n"
      "buffer_load_dwordx4 v12, s[28:31], s43 offen offset:2048 lds\n"
      "buffer_load_dwordx4 v13, s[28:31], s43 offen offset:3072 lds\n"
      "s_waitcnt vmcnt(24)\n"
      "ds_read_b64_tr_b16 v[96:97], v24 offset:0\n"
      "ds_read_b64_tr_b16 v[98:99], v24 offset:512\n"
      "ds_read_b64_tr_b16 v[100:101], v25 offset:0\n"
      "ds_read_b64_tr_b16 v[102:103], v25 offset:512\n"
      "ds_read_b64_tr_b16 v[104:105], v26 offset:0\n"
      "ds_read_b64_tr_b16 v[106:107], v26 offset:512\n"
      "ds_read_b64_tr_b16 v[108:109], v27 offset:0\n"
      "ds_read_b64_tr_b16 v[110:111], v27 offset:512\n"
      "s_waitcnt lgkmcnt(0)\n"
      "s_add_u32 m0, s47, 0x20000\n"
      "s_lshl_b32 s43, s64, 16\n"
      "s_add_u32 s43, s43, s48\n"
      "s_add_u32 s43, s43, 0x8000\n"
      "buffer_load_dwordx4 v10, s[28:31], s43 offen lds\n"
      "buffer_load_dwordx4 v11, s[28:31], s43 offen offset:1024 lds\n"
      "buffer_load_dwordx4 v12, s[28:31], s43 offen offset:2048 lds\n"
      "buffer_load_dwordx4 v13, s[28:31], s43 offen offset:3072 lds\n"
      "s_waitcnt vmcnt(24)\n"
      "ds_read_b64_tr_b16 v[112:113], v24 offset:16384\n"
      "ds_read_b64_tr_b16 v[114:115], v24 offset:16896\n"
      "ds_read_b64_tr_b16 v[116:117], v25 offset:16384\n"
      "ds_read_b64_tr_b16 v[118:119], v25 offset:16896\n"
      "ds_read_b64_tr_b16 v[120:121], v26 offset:16384\n"
      "ds_read_b64_tr_b16 v[122:123], v26 offset:16896\n"
      "ds_read_b64_tr_b16 v[124:125], v27 offset:16384\n"
      "ds_read_b64_tr_b16 v[126:127], v27 offset:16896\n"
      "s_waitcnt vmcnt(20)\n"
      "ds_read_b64_tr_b16 v[128:129], v16 offset:32768\n"
      "ds_read_b64_tr_b16 v[130:131], v16 offset:33280\n"
      "ds_read_b64_tr_b16 v[132:133], v17 offset:32768\n"
      "ds_read_b64_tr_b16 v[134:135], v17 offset:33280\n"
      "ds_read_b64_tr_b16 v[136:137], v18 offset:32768\n"
      "ds_read_b64_tr_b16 v[138:139], v18 offset:33280\n"
      "ds_read_b64_tr_b16 v[140:141], v19 offset:32768\n"
      "ds_read_b64_tr_b16 v[142:143], v19 offset:33280\n"
      "s_branch .LL_P2_%=\n"
      ".p2align 12\n"
      ".LL_P2_%=:\n"
      "s_load_dwordx16 s[80:95], s[74:75], 0x0 glc\n"
      "s_mov_b32 s54, 0x200000\n"
      "v_add_u32 v2, s47, v15\n"
      "s_xor_b32 s42, s47, 0x1000\n"
      "v_add_u32 v3, s42, v15\n"
      "s_xor_b32 s42, s47, 0x2000\n"
      "v_add_u32 v4, s42, v15\n"
      "s_xor_b32 s42, s47, 0x3000\n"
      "v_add_u32 v5, s42, v15\n"
      "s_lshl2_add_u32 s42, s50, s55\n"
      "s_lshl_b32 s42, s42, 14\n"
      "s_lshl_b32 s43, s51, 7\n"
      "s_add_u32 s42, s42, s43\n"
      "v_lshl_add_u32 v6, v31, 11, s42\n"
      "v_bfe_u32 v30, v1, 3, 1\n"
      "v_lshl_or_b32 v6, v30, 10, v6\n"
      "v_bfe_u32 v30, v1, 4, 1\n"
      "v_lshl_or_b32 v6, v30, 5, v6\n"
      "v_lshrrev_b32 v30, 5, v1\n"
      "v_lshl_or_b32 v6, v30, 4, v6\n"
      "s_lshl_b32 s20, s65, 16\n"
      "s_add_u32 s20, s20, s48\n"
      "s_add_u32 s20, s20, 0x8000\n"
      "s_lshl_b32 s21, s66, 16\n"
      "s_add_u32 s21, s21, s48\n"
      "s_add_u32 s21, s21, 0x8000\n"
      "s_lshl_b32 s60, s60, 13\n"
      "s_add_u32 s60, s60, s49\n"
      "s_lshl_b32 s61, s61, 13\n"
      "s_add_u32 s61, s61, s49\n"
      "s_lshl_b32 s62, s62, 13\n"
      "s_add_u32 s62, s62, s49\n"
      "s_lshl_b32 s63, s63, 13\n"
      "s_add_u32 s63, s63, s49\n"
      "s_lshl_b32 s64, s64, 13\n"
      "s_add_u32 s64, s64, s49\n"
      "s_lshl_b32 s65, s65, 13\n"
      "s_add_u32 s65, s65, s49\n"
      "s_lshl_b32 s66, s66, 13\n"
      "s_add_u32 s66, s66, s49\n"
      "v_add_u32 v7, 0x1000, v15\n"
      "s_waitcnt lgkmcnt(0)\n"
      "s_add_u32 s52, s94, 0x7f4a7c15\n"
      "s_addc_u32 s53, s95, 0x9e3779b9\n"
      "s_or_b32 s56, s52, 0xff\n"
      "s_mov_b32 s57, s53\n"
      "v_mov_b32 v30, s52\n"
      "v_mov_b32 v31, s53\n"
      "s_cmp_gt_u32 s19, 27\n"
      "s_cbranch_scc1 .LSPIN0_%=\n"
      "s_barrier\n"
      "s_load_dwordx16 s[80:95], s[74:75], 0x0 glc\n"
      "s_add_u32 m0, s47, 0x24000\n"
      "s_nop 0\n"
      "buffer_load_dwordx4 v10, s[28:31], s20 offen lds\n"
      "buffer_load_dwordx4 v11, s[28:31], s20 offen offset:1024 lds\n"
      "buffer_load_dwordx4 v12, s[28:31], s20 offen offset:2048 lds\n"
      "buffer_load_dwordx4 v13, s[28:31], s20 offen offset:3072 lds\n"
      "s_add_u32 m0, s47, 0x8000\n"
      "s_nop 0\n"
      "buffer_load_dwordx4 v10, s[28:31], s21 offen lds\n"
      "buffer_load_dwordx4 v11, s[28:31], s21 offen offset:1024 lds\n"
      "buffer_load_dwordx4 v12, s[28:31], s21 offen offset:2048 lds\n"
      "buffer_load_dwordx4 v13, s[28:31], s21 offen offset:3072 lds\n"
      "s_branch .LSPIN0_W_%=\n"
      ".LSPIN0_%=:\n"
      "s_load_dwordx16 s[80:95], s[74:75], 0x0 glc\n"
      "s_sub_u32 s54, s54, 1\n"
      "s_cbranch_scc1 .LABORT_%=\n"
      ".LSPIN0_W_%=:\n"
      "s_waitcnt lgkmcnt(0)\n"
      "s_xor_b64 s[42:43], s[80:81], s[56:57]\n"
      "s_xor_b64 s[44:45], s[82:83], s[56:57]\n"
      "s_or_b64 s[42:43], s[42:43], s[44:45]\n"
      "s_xor_b64 s[44:45], s[84:85], s[56:57]\n"
      "s_or_b64 s[42:43], s[42:43], s[44:45]\n"
      "s_xor_b64 s[44:45], s[86:87], s[56:57]\n"
      "s_or_b64 s[42:43], s[42:43], s[44:45]\n"
      "s_xor_b64 s[44:45], s[88:89], s[56:57]\n"
      "s_or_b64 s[42:43], s[42:43], s[44:45]\n"
      "s_xor_b64 s[44:45], s[90:91], s[56:57]\n"
      "s_or_b64 s[42:43], s[42:43], s[44:45]\n"
      "s_xor_b64 s[44:45], s[92:93], s[56:57]\n"
      "s_or_b64 s[42:43], s[42:43], s[44:45]\n"
      "s_andn2_b64 s[42:43], s[42:43], 0xfe\n"
      "s_cbranch_scc1 .LSPIN0_%=\n"
      "buffer_load_dwordx4 v[208:211], v15, s[76:79], s60 offen sc1\n"
      "buffer_load_dwordx4 v[212:215], v15, s[76:79], s61 offen sc1\n"
      "buffer_load_dwordx4 v[216:219], v15, s[76:79], s62 offen sc1\n"
      "buffer_load_dwordx4 v[220:223], v15, s[76:79], s63 offen sc1\n"
      "buffer_load_dwordx4 v[224:227], v15, s[76:79], s64 offen sc1\n"
      "s_load_dwordx16 s[80:95], s[74:75], 0x0 glc\n"
      "buffer_load_dwordx4 v[228:231], v15, s[76:79], s65 offen sc1\n"
      "buffer_load_dwordx4 v[232:235], v15, s[76:79], s66 offen sc1\n"
      "buffer_load_dwordx4 v[236:239], v15, s[76:79], s60 offen offset:1024 sc1\n"
      "buffer_load_dwordx4 v[240:243], v15, s[76:79], s61 offen offset:1024 sc1\n"
      "buffer_load_dwordx4 v[244:247], v15, s[76:79], s62 offen offset:1024 sc1\n"
      "s_waitcnt vmcnt(9)\n"
      "v_mfma_f32_16x16x32_bf16 v[144:147], v[32:35], v[208:211], 0\n"
      "s_waitcnt vmcnt(8)\n"
      "v_mfma_f32_16x16x32_bf16 v[144:147], v[48:51], v[212:215], v[144:147]\n"
      "buffer_load_dwordx4 v[248:251], v15, s[76:79], s63 offen offset:1024 sc1\n"
      "v_mfma_f32_16x16x32_bf16 v[148:151], v[36:39], v[208:211], 0\n"
      "v_mfma_f32_16x16x32_bf16 v[148:151], v[52:55], v[212:215], v[148:151]\n"
      "v_mfma_f32_16x16x32_bf16 v[152:155], v[40:43], v[208:211], 0\n"
      "v_mfma_f32_16x16x32_bf16 v[152:155], v[56:59], v[212:215], v[152:155]\n"
      "buffer_load_dwordx4 v[252:255], v15, s[76:79], s64 offen offset:1024 sc1\n"
      "v_mfma_f32_16x16x32_bf16 v[156:159], v[44:47], v[208:211], 0\n"
      "v_mfma_f32_16x16x32_bf16 v[156:159], v[60:63], v[212:215], v[156:159]\n"
      "s_waitcnt vmcnt(8)\n"
      "v_mfma_f32_16x16x32_bf16 v[144:147], v[64:67], v[216:219], v[144:147]\n"
      "v_mfma_f32_16x16x32_bf16 v[144:147], v[80:83], v[220:223], v[144:147]\n"
      "buffer_load_dwordx4 v[208:211], v15, s[76:79], s65 offen offset:1024 sc1\n"
      "v_mfma_f32_16x16x32_bf16 v[148:151], v[68:71], v[216:219], v[148:151]\n"
      "v_mfma_f32_16x16x32_bf16 v[148:151], v[84:87], v[220:223], v[148:151]\n"
      "v_mfma_f32_16x16x32_bf16 v[152:155], v[72:75], v[216:219], v[152:155]\n"
      "v_mfma_f32_16x16x32_bf16 v[152:155], v[88:91], v[220:223], v[152:155]\n"
      "buffer_load_dwordx4 v[212:215], v15, s[76:79], s66 offen offset:1024 sc1\n"
      "v_mfma_f32_16x16x32_bf16 v[156:159], v[76:79], v[216:219], v[156:159]\n"
      "v_mfma_f32_16x16x32_bf16 v[156:159], v[92:95], v[220:223], v[156:159]\n"
      "s_waitcnt vmcnt(8)\n"
      "v_mfma_f32_16x16x32_bf16 v[144:147], v[96:99], v[224:227], v[144:147]\n"
      "v_mfma_f32_16x16x32_bf16 v[144:147], v[112:115], v[228:231], v[144:147]\n"
      "s_and_b32 s42, s80, s82\n"
      "s_and_b32 s42, s42, s84\n"
      "s_and_b32 s42, s42, s86\n"
      "s_and_b32 s42, s42, s88\n"
      "s_and_b32 s42, s42, s90\n"
      "s_and_b32 s42, s42, s92\n"
      "s_bitcmp0_b32 s42, 1\n"
      "s_cbranch_scc1 .LSPIN1_%=\n"
      ".LSPIN1_X_%=:\n"
      "buffer_load_dwordx4 v[216:219], v15, s[76:79], s60 offen offset:2048 sc1\n"
      "v_mfma_f32_16x16x32_bf16 v[148:151], v[100:103], v[224:227], v[148:151]\n"
      "v_mfma_f32_16x16x32_bf16 v[148:151], v[116:119], v[228:231], v[148:151]\n"
      "v_mfma_f32_16x16x32_bf16 v[152:155], v[104:107], v[224:227], v[152:155]\n"
      "v_mfma_f32_16x16x32_bf16 v[152:155], v[120:123], v[228:231], v[152:155]\n"
      "buffer_load_dwordx4 v[220:223], v15, s[76:79], s61 offen offset:2048 sc1\n"
      "v_mfma_f32_16x16x32_bf16 v[156:159], v[108:111], v[224:227], v[156:159]\n"
      "v_mfma_f32_16x16x32_bf16 v[156:159], v[124:127], v[228:231], v[156:159]\n"
      "s_waitcnt vmcnt(9)\n"
      "v_mfma_f32_16x16x32_bf16 v[144:147], v[128:131], v[232:235], v[144:147]\n"
      "buffer_load_dwordx4 v[224:227], v15, s[76:79], s60 offen offset:3072 sc1\n"
      "v_mfma_f32_16x16x32_bf16 v[148:151], v[132:135], v[232:235], v[148:151]\n"
      "v_mfma_f32_16x16x32_bf16 v[152:155], v[136:139], v[232:235], v[152:155]\n"
      "v_mfma_f32_16x16x32_bf16 v[156:159], v[140:143], v[232:235], v[156:159]\n"
      "s_waitcnt vmcnt(8)\n"
      "v_mfma_f32_16x16x32_bf16 v[160:163], v[32:35], v[236:239], 0\n"
      "v_mfma_f32_16x16x32_bf16 v[160:163], v[48:51], v[240:243], v[160:163]\n"
      "buffer_load_dwordx4 v[228:231], v15, s[76:79], s61 offen offset:3072 sc1\n"
      "v_mfma_f32_16x16x32_bf16 v[164:167], v[36:39], v[236:239], 0\n"
      "v_mfma_f32_16x16x32_bf16 v[164:167], v[52:55], v[240:243], v[164:167]\n"
      "v_mfma_f32_16x16x32_bf16 v[168:171], v[40:43], v[236:239], 0\n"
      "v_mfma_f32_16x16x32_bf16 v[168:171], v[56:59], v[240:243], v[168:171]\n"
      "buffer_load_dwordx4 v[232:235], v15, s[76:79], s62 offen offset:2048 sc1\n"
      "s_load_dwordx16 s[80:95], s[74:75], 0x0 glc\n"
      "v_mfma_f32_16x16x32_bf16 v[172:175], v[44:47], v[236:239], 0\n"
      "v_mfma_f32_16x16x32_bf16 v[172:175], v[60:63], v[240:243], v[172:175]\n"
      "s_waitcnt vmcnt(8)\n"
      "v_mfma_f32_16x16x32_bf16 v[160:163], v[64:67], v[244:247], v[160:163]\n"
      "v_mfma_f32_16x16x32_bf16 v[160:163], v[80:83], v[248:251], v[160:163]\n"
      "buffer_load_dwordx4 v[236:239], v15, s[76:79], s63 offen offset:2048 sc1\n"
      "v_mfma_f32_16x16x32_bf16 v[164:167], v[68:71], v[244:247], v[164:167]\n"
      "v_mfma_f32_16x16x32_bf16 v[164:167], v[84:87], v[248:251], v[164:167]\n"
      "v_mfma_f32_16x16x32_bf16 v[168:171], v[72:75], v[244:247], v[168:171]\n"
      "v_mfma_f32_16x16x32_bf16 v[168:171], v[88:91], v[248:251], v[168:171]\n"
      "buffer_load_dwordx4 v[240:243], v15, s[76:79], s62 offen offset:3072 sc1\n"
      "v_mfma_f32_16x16x32_bf16 v[172:175], v[76:79], v[244:247], v[172:175]\n"
      "v_mfma_f32_16x16x32_bf16 v[172:175], v[92:95], v[248:251], v[172:175]\n"
      "s_waitcnt vmcnt(8)\n"
      "v_mfma_f32_16x16x32_bf16 v[160:163], v[96:99], v[252:255], v[160:163]\n"
      "v_mfma_f32_16x16x32_bf16 v[160:163], v[112:115], v[208:211], v[160:163]\n"
      "buffer_load_dwordx4 v[244:247], v15, s[76:79], s63 offen offset:3072 sc1\n"
      "v_mfma_f32_16x16x32_bf16 v[164:167], v[100:103], v[252:255], v[164:167]\n"
      "v_mfma_f32_16x16x32_bf16 v[164:167], v[116:119], v[208:211], v[164:167]\n"
      "v_mfma_f32_16x16x32_bf16 v[168:171], v[104:107], v[252:255], v[168:171]\n"
      "v_mfma_f32_16x16x32_bf16 v[168:171], v[120:123], v[208:211], v[168:171]\n"
      "buffer_load_dwordx4 v[248:251], v15, s[76:79], s64 offen offset:2048 sc1\n"
      "v_mfma_f32_16x16x32_bf16 v[172:175], v[108:111], v[252:255], v[172:175]\n"
      "v_mfma_f32_16x16x32_bf16 v[172:175], v[124:127], v[208:211], v[172:175]\n"
      "s_waitcnt vmcnt(9)\n"
      "v_mfma_f32_16x16x32_bf16 v[160:163], v[128:131], v[212:215], v[160:163]\n"
      "buffer_load_dwordx4 v[252:255], v15, s[76:79], s65 offen offset:2048 sc1\n"
      "v_mfma_f32_16x16x32_bf16 v[164:167], v[132:135], v[212:215], v[164:167]\n"
      "v_mfma_f32_16x16x32_bf16 v[168:171], v[136:139], v[212:215], v[168:171]\n"
      "v_mfma_f32_16x16x32_bf16 v[172:175], v[140:143], v[212:215], v[172:175]\n"
      "s_waitcnt vmcnt(8)\n"
      "v_mfma_f32_16x16x32_bf16 v[176:179], v[32:35], v[216:219], 0\n"
      "v_mfma_f32_16x16x32_bf16 v[176:179], v[48:51], v[220:223], v[176:179]\n"
      "buffer_load_dwordx4 v[208:211], v15, s[76:79], s64 offen offset:3072 sc1\n"
      "v_mfma_f32_16x16x32_bf16 v[180:183], v[36:39], v[216:219], 0\n"
      "v_mfma_f32_16x16x32_bf16 v[180:183], v[52:55], v[220:223], v[180:183]\n"
      "v_mfma_f32_16x16x32_bf16 v[184:187], v[40:43], v[216:219], 0\n"
      "v_mfma_f32_16x16x32_bf16 v[184:187], v[56:59], v[220:223], v[184:187]\n"
      "buffer_load_dwordx4 v[212:215], v15, s[76:79], s65 offen offset:3072 sc1\n"
      "v_mfma_f32_16x16x32_bf16 v[188:191], v[44:47], v[216:219], 0\n"
      "v_mfma_f32_16x16x32_bf16 v[188:191], v[60:63], v[220:223], v[188:191]\n"
      "s_waitcnt vmcnt(8)\n"
      "v_mfma_f32_16x16x32_bf16 v[204:207], v[44:47], v[224:227], 0\n"
      "v_mfma_f32_16x16x32_bf16 v[204:207], v[60:63], v[228:231], v[204:207]\n"
      "buffer_load_dwordx4 v[216:219], v15, s[76:79], s66 offen offset:2048 sc1\n"
      "v_mfma_f32_16x16x32_bf16 v[200:203], v[40:43], v[224:227], 0\n"
      "v_mfma_f32_16x16x32_bf16 v[200:203], v[56:59], v[228:231], v[200:203]\n"
      "v_mfma_f32_16x16x32_bf16 v[196:199], v[36:39], v[224:227], 0\n"
      "v_mfma_f32_16x16x32_bf16 v[196:199], v[52:55], v[228:231], v[196:199]\n"
      "buffer_load_dwordx4 v[220:223], v15, s[76:79], s66 offen offset:3072 sc1\n"
      "v_mfma_f32_16x16x32_bf16 v[192:195], v[32:35], v[224:227], 0\n"
      "v_mfma_f32_16x16x32_bf16 v[192:195], v[48:51], v[228:231], v[192:195]\n"
      "s_waitcnt vmcnt(8)\n"
      "v_mfma_f32_16x16x32_bf16 v[176:179], v[64:67], v[232:235], v[176:179]\n"
      "ds_read_b64_tr_b16 v[32:33], v20 offset:0\n"
      "ds_read_b64_tr_b16 v[34:35], v20 offset:512\n"
      "v_mfma_f32_16x16x32_bf16 v[176:179], v[80:83], v[236:239], v[176:179]\n"
      "s_and_b32 s42, s80, s82\n"
      "s_and_b32 s42, s42, s84\n"
      "s_and_b32 s42, s42, s86\n"
      "s_and_b32 s42, s42, s88\n"
      "s_and_b32 s42, s42, s90\n"
      "s_and_b32 s42, s42, s92\n"
      "s_bitcmp0_b32 s42, 2\n"
      "s_cbranch_scc1 .LSPIN2_%=\n"
      ".LSPIN2_X_%=:\n"
      "buffer_load_dwordx4 v[224:227], v7, s[76:79], s60 offen offset:1024 sc1\n"
      "v_mfma_f32_16x16x32_bf16 v[180:183], v[68:71], v[232:235], v[180:183]\n"
      "ds_read_b64_tr_b16 v[36:37], v21 offset:0\n"
      "ds_read_b64_tr_b16 v[38:39], v21 offset:512\n"
      "v_mfma_f32_16x16x32_bf16 v[180:183], v[84:87], v[236:239], v[180:183]\n"
      "ds_read_b64_tr_b16 v[40:41], v22 offset:0\n"
      "ds_read_b64_tr_b16 v[42:43], v22 offset:512\n"
      "v_mfma_f32_16x16x32_bf16 v[184:187], v[72:75], v[232:235], v[184:187]\n"
      "ds_read_b64_tr_b16 v[44:45], v23 offset:0\n"
      "ds_read_b64_tr_b16 v[46:47], v23 offset:512\n"
      "v_mfma_f32_16x16x32_bf16 v[184:187], v[88:91], v[236:239], v[184:187]\n"
      "buffer_load_dwordx4 v[228:231], v7, s[76:79], s61 offen offset:1024 sc1\n"
      "v_mfma_f32_16x16x32_bf16 v[188:191], v[76:79], v[232:235], v[188:191]\n"
      "ds_read_b64_tr_b16 v[48:49], v20 offset:16384\n"
      "ds_read_b64_tr_b16 v[50:51], v20 offset:16896\n"
      "v_mfma_f32_16x16x32_bf16 v[188:191], v[92:95], v[236:239], v[188:191]\n"
      "ds_read_b64_tr_b16 v[52:53], v21 offset:16384\n"
      "s_waitcnt vmcnt(8)\n"
      "v_mfma_f32_16x16x32_bf16 v[204:207], v[76:79], v[240:243], v[204:207]\n"
      "ds_read_b64_tr_b16 v[54:55], v21 offset:16896\n"
      "v_mfma_f32_16x16x32_bf16 v[204:207], v[92:95], v[244:247], v[204:207]\n"
      "buffer_load_dwordx4 v[232:235], v7, s[76:79], s62 offen offset:1024 sc1\n"
      "v_mfma_f32_16x16x32_bf16 v[200:203], v[72:75], v[240:243], v[200:203]\n"
      "ds_read_b64_tr_b16 v[56:57], v22 offset:16384\n"
      "v_mfma_f32_16x16x32_bf16 v[200:203], v[88:91], v[244:247], v[200:203]\n"
      "ds_read_b64_tr_b16 v[58:59], v22 offset:16896\n"
      "v_mfma_f32_16x16x32_bf16 v[196:199], v[68:71], v[240:243], v[196:199]\n"
      "ds_read_b64_tr_b16 v[60:61], v23 offset:16384\n"
      "v_mfma_f32_16x16x32_bf16 v[196:199], v[84:87], v[244:247], v[196:199]\n"
      "buffer_load_dwordx4 v[236:239], v7, s[76:79], s63 offen offset:1024 sc1\n"
      "v_mfma_f32_16x16x32_bf16 v[192:195], v[64:67], v[240:243], v[192:195]\n"
      "ds_read_b64_tr_b16 v[62:63], v23 offset:16896\n"
      "v_mfma_f32_16x16x32_bf16 v[192:195], v[80:83], v[244:247], v[192:195]\n"
      "s_waitcnt vmcnt(8)\n"
      "v_mfma_f32_16x16x32_bf16 v[176:179], v[96:99], v[248:251], v[176:179]\n"
      "ds_read_b64_tr_b16 v[64:65], v16 offset:0\n"
      "ds_read_b64_tr_b16 v[66:67], v16 offset:512\n"
      "v_mfma_f32_16x16x32_bf16 v[176:179], v[112:115], v[252:255], v[176:179]\n"
      "buffer_load_dwordx4 v[240:243], v7, s[76:79], s64 offen offset:1024 sc1\n"
      "v_mfma_f32_16x16x32_bf16 v[180:183], v[100:103], v[248:251], v[180:183]\n"
      "ds_read_b64_tr_b16 v[68:69], v17 offset:0\n"
      "ds_read_b64_tr_b16 v[70:71], v17 offset:512\n"
      "v_mfma_f32_16x16x32_bf16 v[180:183], v[116:119], v[252:255], v[180:183]\n"
      "ds_read_b64_tr_b16 v[72:73], v18 offset:0\n"
      "ds_read_b64_tr_b16 v[74:75], v18 offset:512\n"
      "v_mfma_f32_16x16x32_bf16 v[184:187], v[104:107], v[248:251], v[184:187]\n"
      "ds_read_b64_tr_b16 v[76:77], v19 offset:0\n"
      "ds_read_b64_tr_b16 v[78:79], v19 offset:512\n"
      "v_mfma_f32_16x16x32_bf16 v[184:187], v[120:123], v[252:255], v[184:187]\n"
      "buffer_load_dwordx4 v[244:247], v7, s[76:79], s65 offen offset:1024 sc1\n"
      "v_mfma_f32_16x16x32_bf16 v[188:191], v[108:111], v[248:251], v[188:191]\n"
      "ds_read_b64_tr_b16 v[80:81], v16 offset:49152\n"
      "ds_read_b64_tr_b16 v[82:83], v16 offset:49664\n"
      "v_mfma_f32_16x16x32_bf16 v[188:191], v[124:127], v[252:255], v[188:191]\n"
      "ds_read_b64_tr_b16 v[84:85], v17 offset:49152\n"
      "s_waitcnt vmcnt(8)\n"
      "v_mfma_f32_16x16x32_bf16 v[204:207], v[108:111], v[208:211], v[204:207]\n"
      "ds_read_b64_tr_b16 v[86:87], v17 offset:49664\n"
      "v_mfma_f32_16x16x32_bf16 v[204:207], v[124:127], v[212:215], v[204:207]\n"
      "buffer_load_dwordx4 v[248:251], v7, s[76:79], s66 offen offset:1024 sc1\n"
      "v_mfma_f32_16x16x32_bf16 v[200:203], v[104:107], v[208:211], v[200:203]\n"
      "ds_read_b64_tr_b16 v[88:89], v18 offset:49152\n"
      "v_mfma_f32_16x16x32_bf16 v[200:203], v[120:123], v[212:215], v[200:203]\n"
      "ds_read_b64_tr_b16 v[90:91], v18 offset:49664\n"
      "v_mfma_f32_16x16x32_bf16 v[196:199], v[100:103], v[208:211], v[196:199]\n"
      "ds_read_b64_tr_b16 v[92:93], v19 offset:49152\n"
      "v_mfma_f32_16x16x32_bf16 v[196:199], v[116:119], v[212:215], v[196:199]\n"
      "buffer_load_dwordx4 v[252:255], v7, s[76:79], s60 offen offset:2048 sc1\n"
      "v_mfma_f32_16x16x32_bf16 v[192:195], v[96:99], v[208:211], v[192:195]\n"
      "ds_read_b64_tr_b16 v[94:95], v19 offset:49664\n"
      "v_mfma_f32_16x16x32_bf16 v[192:195], v[112:115], v[212:215], v[192:195]\n"
      "s_waitcnt vmcnt(9)\n"
      "v_mfma_f32_16x16x32_bf16 v[176:179], v[128:131], v[216:219], v[176:179]\n"
      "buffer_load_dwordx4 v[208:211], v7, s[76:79], s61 offen offset:2048 sc1\n"
      "v_mfma_f32_16x16x32_bf16 v[180:183], v[132:135], v[216:219], v[180:183]\n"
      "ds_read_b64_tr_b16 v[96:97], v24 offset:0\n"
      "ds_read_b64_tr_b16 v[98:99], v24 offset:512\n"
      "v_mfma_f32_16x16x32_bf16 v[184:187], v[136:139], v[216:219], v[184:187]\n"
      "ds_read_b64_tr_b16 v[100:101], v25 offset:0\n"
      "ds_read_b64_tr_b16 v[102:103], v25 offset:512\n"
      "v_mfma_f32_16x16x32_bf16 v[188:191], v[140:143], v[216:219], v[188:191]\n"
      "ds_read_b64_tr_b16 v[104:105], v26 offset:0\n"
      "ds_read_b64_tr_b16 v[106:107], v26 offset:512\n"
      "s_waitcnt vmcnt(9)\n"
      "v_mfma_f32_16x16x32_bf16 v[204:207], v[140:143], v[220:223], v[204:207]\n"
      "buffer_load_dwordx4 v[212:215], v7, s[76:79], s62 offen offset:2048 sc1\n"
      "v_mfma_f32_16x16x32_bf16 v[200:203], v[136:139], v[220:223], v[200:203]\n"
      "ds_read_b64_tr_b16 v[108:109], v27 offset:0\n"
      "ds_read_b64_tr_b16 v[110:111], v27 offset:512\n"
      "v_mfma_f32_16x16x32_bf16 v[196:199], v[132:135], v[220:223], v[196:199]\n"
      "ds_read_b64_tr_b16 v[112:113], v24 offset:16384\n"
      "ds_read_b64_tr_b16 v[114:115], v24 offset:16896\n"
      "v_mfma_f32_16x16x32_bf16 v[192:195], v[128:131], v[220:223], v[192:195]\n"
      "ds_read_b64_tr_b16 v[116:117], v25 offset:16384\n"
      "s_waitcnt lgkmcnt(8)\n"
      "s_waitcnt vmcnt(8)\n"
      "v_mfma_f32_16x16x32_bf16 v[160:163], v[32:35], v[224:227], v[160:163]\n"
      "ds_read_b64_tr_b16 v[118:119], v25 offset:16896\n"
      "ds_read_b64_tr_b16 v[120:121], v26 offset:16384\n"
      "v_mfma_f32_16x16x32_bf16 v[160:163], v[48:51], v[228:231], v[160:163]\n"
      "buffer_load_dwordx4 v[216:219], v7, s[76:79], s63 offen offset:2048 sc1\n"
      "v_mfma_f32_16x16x32_bf16 v[164:167], v[36:39], v[224:227], v[164:167]\n"
      "ds_read_b64_tr_b16 v[122:123], v26 offset:16896\n"
      "ds_read_b64_tr_b16 v[124:125], v27 offset:16384\n"
      "v_mfma_f32_16x16x32_bf16 v[164:167], v[52:55], v[228:231], v[164:167]\n"
      "ds_read_b64_tr_b16 v[126:127], v27 offset:16896\n"
      "ds_read_b64_tr_b16 v[128:129], v16 offset:32768\n"
      "v_mfma_f32_16x16x32_bf16 v[168:171], v[40:43], v[224:227], v[168:171]\n"
      "ds_read_b64_tr_b16 v[130:131], v16 offset:33280\n"
      "ds_read_b64_tr_b16 v[132:133], v17 offset:32768\n"
      "v_mfma_f32_16x16x32_bf16 v[168:171], v[56:59], v[228:231], v[168:171]\n"
      "buffer_load_dwordx4 v[220:223], v7, s[76:79], s64 offen offset:2048 sc1\n"
      "v_mfma_f32_16x16x32_bf16 v[172:175], v[44:47], v[224:227], v[172:175]\n"
      "ds_read_b64_tr_b16 v[134:135], v17 offset:33280\n"
      "v_mfma_f32_16x16x32_bf16 v[172:175], v[60:63], v[228:231], v[172:175]\n"
      "ds_read_b64_tr_b16 v[136:137], v18 offset:32768\n"
      "s_waitcnt lgkmcnt(8)\n"
      "s_waitcnt vmcnt(8)\n"
      "v_mfma_f32_16x16x32_bf16 v[160:163], v[64:67], v[232:235], v[160:163]\n"
      "ds_read_b64_tr_b16 v[138:139], v18 offset:33280\n"
      "v_mfma_f32_16x16x32_bf16 v[160:163], v[80:83], v[236:239], v[160:163]\n"
      "buffer_load_dwordx4 v[224:227], v7, s[76:79], s65 offen offset:2048 sc1\n"
      "v_mfma_f32_16x16x32_bf16 v[164:167], v[68:71], v[232:235], v[164:167]\n"
      "ds_read_b64_tr_b16 v[140:141], v19 offset:32768\n"
      "v_mfma_f32_16x16x32_bf16 v[164:167], v[84:87], v[236:239], v[164:167]\n"
      "ds_read_b64_tr_b16 v[142:143], v19 offset:33280\n"
      "v_mfma_f32_16x16x32_bf16 v[168:171], v[72:75], v[232:235], v[168:171]\n"
      "v_mfma_f32_16x16x32_bf16 v[168:171], v[88:91], v[236:239], v[168:171]\n"
      "buffer_load_dwordx4 v[228:231], v7, s[76:79], s66 offen offset:2048 sc1\n"
      "v_mfma_f32_16x16x32_bf16 v[172:175], v[76:79], v[232:235], v[172:175]\n"
      "v_mfma_f32_16x16x32_bf16 v[172:175], v[92:95], v[236:239], v[172:175]\n"
      "s_waitcnt lgkmcnt(8)\n"
      "s_waitcnt vmcnt(8)\n"
      "v_mfma_f32_16x16x32_bf16 v[160:163], v[96:99], v[240:243], v[160:163]\n"
      "v_mfma_f32_16x16x32_bf16 v[160:163], v[112:115], v[244:247], v[160:163]\n"
      "buffer_load_dwordx4 v[232:235], v7, s[76:79], s60 offen offset:3072 sc1\n"
      "v_mfma_f32_16x16x32_bf16 v[164:167], v[100:103], v[240:243], v[164:167]\n"
      "v_mfma_f32_16x16x32_bf16 v[164:167], v[116:119], v[244:247], v[164:167]\n"
      "v_mfma_f32_16x16x32_bf16 v[168:171], v[104:107], v[240:243], v[168:171]\n"
      "v_mfma_f32_16x16x32_bf16 v[168:171], v[120:123], v[244:247], v[168:171]\n"
      "buffer_load_dwordx4 v[236:239], v7, s[76:79], s61 offen offset:3072 sc1\n"
      "v_mfma_f32_16x16x32_bf16 v[172:175], v[108:111], v[240:243], v[172:175]\n"
      "v_mfma_f32_16x16x32_bf16 v[172:175], v[124:127], v[244:247], v[172:175]\n"
      "s_waitcnt lgkmcnt(0)\n"
      "s_waitcnt vmcnt(9)\n"
      "v_mfma_f32_16x16x32_bf16 v[160:163], v[128:131], v[248:251], v[160:163]\n"
      "buffer_load_dwordx4 v[240:243], v7, s[76:79], s62 offen offset:3072 sc1\n"
      "v_mfma_f32_16x16x32_bf16 v[164:167], v[132:135], v[248:251], v[164:167]\n"
      "v_mfma_f32_16x16x32_bf16 v[168:171], v[136:139], v[248:251], v[168:171]\n"
      "v_mfma_f32_16x16x32_bf16 v[172:175], v[140:143], v[248:251], v[172:175]\n"
      "s_waitcnt vmcnt(8)\n"
      "v_mfma_f32_16x16x32_bf16 v[176:179], v[32:35], v[252:255], v[176:179]\n"
      "v_mfma_f32_16x16x32_bf16 v[176:179], v[48:51], v[208:211], v[176:179]\n"
      "buffer_load_dwordx4 v[244:247], v7, s[76:79], s63 offen offset:3072 sc1\n"
      "v_mfma_f32_16x16x32_bf16 v[180:183], v[36:39], v[252:255], v[180:183]\n"
      "v_mfma_f32_16x16x32_bf16 v[180:183], v[52:55], v[208:211], v[180:183]\n"
      "v_mfma_f32_16x16x32_bf16 v[184:187], v[40:43], v[252:255], v[184:187]\n"
      "v_mfma_f32_16x16x32_bf16 v[184:187], v[56:59], v[208:211], v[184:187]\n"
      "buffer_load_dwordx4 v[248:251], v7, s[76:79], s64 offen offset:3072 sc1\n"
      "v_mfma_f32_16x16x32_bf16 v[188:191], v[44:47], v[252:255], v[188:191]\n"
      "v_mfma_f32_16x16x32_bf16 v[188:191], v[60:63], v[208:211], v[188:191]\n"
      "s_waitcnt vmcnt(8)\n"
      "v_mfma_f32_16x16x32_bf16 v[176:179], v[64:67], v[212:215], v[176:179]\n"
      "ds_write_b128 v2, v[160:163] offset:0\n"
      "v_mfma_f32_16x16x32_bf16 v[176:179], v[80:83], v[216:219], v[176:179]\n"
      "buffer_load_dwordx4 v[252:255], v7, s[76:79], s65 offen offset:3072 sc1\n"
      "v_mfma_f32_16x16x32_bf16 v[180:183], v[68:71], v[212:215], v[180:183]\n"
      "ds_write_b128 v2, v[164:167] offset:1024\n"
      "v_mfma_f32_16x16x32_bf16 v[180:183], v[84:87], v[216:219], v[180:183]\n"
      "ds_write_b128 v2, v[168:171] offset:2048\n"
      "v_mfma_f32_16x16x32_bf16 v[184:187], v[72:75], v[212:215], v[184:187]\n"
      "ds_write_b128 v2, v[172:175] offset:3072\n"
      "v_mfma_f32_16x16x32_bf16 v[184:187], v[88:91], v[216:219], v[184:187]\n"
      "buffer_load_dwordx4 v[208:211], v7, s[76:79], s66 offen offset:3072 sc1\n"
      "v_mfma_f32_16x16x32_bf16 v[188:191], v[76:79], v[212:215], v[188:191]\n"
      "v_mfma_f32_16x16x32_bf16 v[188:191], v[92:95], v[216:219], v[188:191]\n"
      "s_waitcnt vmcnt(8)\n"
      "v_mfma_f32_16x16x32_bf16 v[176:179], v[96:99], v[220:223], v[176:179]\n"
      "v_mfma_f32_16x16x32_bf16 v[176:179], v[112:115], v[224:227], v[176:179]\n"
      "buffer_load_dwordx4 v[212:215], v7, s[76:79], s60 offen sc1\n"
      "v_mfma_f32_16x16x32_bf16 v[180:183], v[100:103], v[220:223], v[180:183]\n"
      "v_mfma_f32_16x16x32_bf16 v[180:183], v[116:119], v[224:227], v[180:183]\n"
      "v_mfma_f32_16x16x32_bf16 v[184:187], v[104:107], v[220:223], v[184:187]\n"
      "v_mfma_f32_16x16x32_bf16 v[184:187], v[120:123], v[224:227], v[184:187]\n"
      "buffer_load_dwordx4 v[216:219], v7, s[76:79], s61 offen sc1\n"
      "v_mfma_f32_16x16x32_bf16 v[188:191], v[108:111], v[220:223], v[188:191]\n"
      "v_mfma_f32_16x16x32_bf16 v[188:191], v[124:127], v[224:227], v[188:191]\n"
      "s_waitcnt vmcnt(9)\n"
      "v_mfma_f32_16x16x32_bf16 v[176:179], v[128:131], v[228:231], v[176:179]\n"
      "buffer_load_dwordx4 v[220:223], v7, s[76:79], s62 offen sc1\n"
      "v_mfma_f32_16x16x32_bf16 v[180:183], v[132:135], v[228:231], v[180:183]\n"
      "v_mfma_f32_16x16x32_bf16 v[184:187], v[136:139], v[228:231], v[184:187]\n"
      "v_mfma_f32_16x16x32_bf16 v[188:191], v[140:143], v[228:231], v[188:191]\n"
      "s_waitcnt vmcnt(8)\n"
      "v_mfma_f32_16x16x32_bf16 v[192:195], v[32:35], v[232:235], v[192:195]\n"
      "v_mfma_f32_16x16x32_bf16 v[192:195], v[48:51], v[236:239], v[192:195]\n"
      "buffer_load_dwordx4 v[224:227], v7, s[76:79], s63 offen sc1\n"
      "v_mfma_f32_16x16x32_bf16 v[196:199], v[36:39], v[232:235], v[196:199]\n"
      "v_mfma_f32_16x16x32_bf16 v[196:199], v[52:55], v[236:239], v[196:199]\n"
      "v_mfma_f32_16x16x32_bf16 v[200:203], v[40:43], v[232:235], v[200:203]\n"
      "v_mfma_f32_16x16x32_bf16 v[200:203], v[56:59], v[236:239], v[200:203]\n"
      "buffer_load_dwordx4 v[228:231], v7, s[76:79], s64 offen sc1\n"
      "v_mfma_f32_16x16x32_bf16 v[204:207], v[44:47], v[232:235], v[204:207]\n"
      "v_mfma_f32_16x16x32_bf16 v[204:207], v[60:63], v[236:239], v[204:207]\n"
      "s_waitcnt vmcnt(8)\n"
      "v_mfma_f32_16x16x32_bf16 v[192:195], v[64:67], v[240:243], v[192:195]\n"
      "ds_write_b128 v2, v[176:179] offset:32768\n"
      "v_mfma_f32_16x16x32_bf16 v[192:195], v[80:83], v[244:247], v[192:195]\n"
      "buffer_load_dwordx4 v[232:235], v7, s[76:79], s65 offen sc1\n"
      "v_mfma_f32_16x16x32_bf16 v[196:199], v[68:71], v[240:243], v[196:199]\n"
      "ds_write_b128 v2, v[180:183] offset:33792\n"
      "v_mfma_f32_16x16x32_bf16 v[196:199], v[84:87], v[244:247], v[196:199]\n"
      "ds_write_b128 v2, v[184:187] offset:34816\n"
      "v_mfma_f32_16x16x32_bf16 v[200:203], v[72:75], v[240:243], v[200:203]\n"
      "ds_write_b128 v2, v[188:191] offset:35840\n"
      "v_mfma_f32_16x16x32_bf16 v[200:203], v[88:91], v[244:247], v[200:203]\n"
      "buffer_load_dwordx4 v[236:239], v7, s[76:79], s66 offen sc1\n"
      "s_mov_b64 exec, 1\n"
      "buffer_store_dwordx2 v[30:31], v14, s[76:79], 0 offen offset:56 sc1\n"
      "s_mov_b64 exec, -1\n"
      "v_mfma_f32_16x16x32_bf16 v[204:207], v[76:79], v[240:243], v[204:207]\n"
      "v_mfma_f32_16x16x32_bf16 v[204:207], v[92:95], v[244:247], v[204:207]\n"
      "s_waitcnt vmcnt(9)\n"
      "v_mfma_f32_16x16x32_bf16 v[192:195], v[96:99], v[248:251], v[192:195]\n"
      "v_mfma_f32_16x16x32_bf16 v[192:195], v[112:115], v[252:255], v[192:195]\n"
      "v_mfma_f32_16x16x32_bf16 v[196:199], v[100:103], v[248:251], v[196:199]\n"
      "v_mfma_f32_16x16x32_bf16 v[196:199], v[116:119], v[252:255], v[196:199]\n"
      "v_mfma_f32_16x16x32_bf16 v[200:203], v[104:107], v[248:251], v[200:203]\n"
      "v_mfma_f32_16x16x32_bf16 v[200:203], v[120:123], v[252:255], v[200:203]\n"
      "v_mfma_f32_16x16x32_bf16 v[204:207], v[108:111], v[248:251], v[204:207]\n"
      "v_mfma_f32_16x16x32_bf16 v[204:207], v[124:127], v[252:255], v[204:207]\n"
      "s_waitcnt vmcnt(8)\n"
      "v_mfma_f32_16x16x32_bf16 v[192:195], v[128:131], v[208:211], v[192:195]\n"
      "v_mfma_f32_16x16x32_bf16 v[196:199], v[132:135], v[208:211], v[196:199]\n"
      "v_mfma_f32_16x16x32_bf16 v[200:203], v[136:139], v[208:211], v[200:203]\n"
      "v_mfma_f32_16x16x32_bf16 v[204:207], v[140:143], v[208:211], v[204:207]\n"
      "s_waitcnt vmcnt(6)\n"
      "v_mfma_f32_16x16x32_bf16 v[144:147], v[32:35], v[212:215], v[144:147]\n"
      "v_mfma_f32_16x16x32_bf16 v[144:147], v[48:51], v[216:219], v[144:147]\n"
      "v_mfma_f32_16x16x32_bf16 v[148:151], v[36:39], v[212:215], v[148:151]\n"
      "v_mfma_f32_16x16x32_bf16 v[148:151], v[52:55], v[216:219], v[148:151]\n"
      "v_mfma_f32_16x16x32_bf16 v[152:155], v[40:43], v[212:215], v[152:155]\n"
      "v_mfma_f32_16x16x32_bf16 v[152:155], v[56:59], v[216:219], v[152:155]\n"
      "v_mfma_f32_16x16x32_bf16 v[156:159], v[44:47], v[212:215], v[156:159]\n"
      "v_mfma_f32_16x16x32_bf16 v[156:159], v[60:63], v[216:219], v[156:159]\n"
      "s_waitcnt vmcnt(4)\n"
      "v_mfma_f32_16x16x32_bf16 v[144:147], v[64:67], v[220:223], v[144:147]\n"
      "ds_write_b128 v2, v[192:195] offset:49152\n"
      "ds_write_b128 v2, v[196:199] offset:50176\n"
      "v_mfma_f32_16x16x32_bf16 v[144:147], v[80:83], v[224:227], v[144:147]\n"
      "ds_write_b128 v2, v[200:203] offset:51200\n"
      "ds_write_b128 v2, v[204:207] offset:52224\n"
      "v_mfma_f32_16x16x32_bf16 v[148:151], v[68:71], v[220:223], v[148:151]\n"
      "v_mfma_f32_16x16x32_bf16 v[148:151], v[84:87], v[224:227], v[148:151]\n"
      "v_mfma_f32_16x16x32_bf16 v[152:155], v[72:75], v[220:223], v[152:155]\n"
      "v_mfma_f32_16x16x32_bf16 v[152:155], v[88:91], v[224:227], v[152:155]\n"
      "v_mfma_f32_16x16x32_bf16 v[156:159], v[76:79], v[220:223], v[156:159]\n"
      "v_mfma_f32_16x16x32_bf16 v[156:159], v[92:95], v[224:227], v[156:159]\n"
      "s_waitcnt lgkmcnt(0)\n"
      "s_barrier\n"
      "s_waitcnt vmcnt(2)\n"
      "v_mfma_f32_16x16x32_bf16 v[144:147], v[96:99], v[228:231], v[144:147]\n"
      "ds_read_b128 v[160:163], v3 offset:0\n"
      "ds_read_b128 v[164:167], v3 offset:1024\n"
      "v_mfma_f32_16x16x32_bf16 v[144:147], v[112:115], v[232:235], v[144:147]\n"
      "ds_read_b128 v[168:171], v3 offset:2048\n"
      "ds_read_b128 v[172:175], v3 offset:3072\n"
      "v_mfma_f32_16x16x32_bf16 v[148:151], v[100:103], v[228:231], v[148:151]\n"
      "ds_read_b128 v[176:179], v4 offset:32768\n"
      "ds_read_b128 v[180:183], v4 offset:33792\n"
      "v_mfma_f32_16x16x32_bf16 v[148:151], v[116:119], v[232:235], v[148:151]\n"
      "ds_read_b128 v[184:187], v4 offset:34816\n"
      "ds_read_b128 v[188:191], v4 offset:35840\n"
      "v_mfma_f32_16x16x32_bf16 v[152:155], v[104:107], v[228:231], v[152:155]\n"
      "ds_read_b128 v[192:195], v5 offset:49152\n"
      "ds_read_b128 v[196:199], v5 offset:50176\n"
      "v_mfma_f32_16x16x32_bf16 v[152:155], v[120:123], v[232:235], v[152:155]\n"
      "ds_read_b128 v[200:203], v5 offset:51200\n"
      "ds_read_b128 v[204:207], v5 offset:52224\n"
      "v_mfma_f32_16x16x32_bf16 v[156:159], v[108:111], v[228:231], v[156:159]\n"
      "v_mfma_f32_16x16x32_bf16 v[156:159], v[124:127], v[232:235], v[156:159]\n"
      "s_waitcnt vmcnt(1)\n"
      "v_mfma_f32_16x16x32_bf16 v[144:147], v[128:131], v[236:239], v[144:147]\n"
      "v_mfma_f32_16x16x32_bf16 v[148:151], v[132:135], v[236:239], v[148:151]\n"
      "v_mfma_f32_16x16x32_bf16 v[152:155], v[136:139], v[236:239], v[152:155]\n"
      "v_mfma_f32_16x16x32_bf16 v[156:159], v[140:143], v[236:239], v[156:159]\n"
      "s_waitcnt lgkmcnt(0)\n"
      "v_pk_add_f32 v[160:161], v[160:161], v[176:177]\n"
      "v_pk_add_f32 v[162:163], v[162:163], v[178:179]\n"
      "v_pk_add_f32 v[164:165], v[164:165], v[180:181]\n"
      "v_pk_add_f32 v[166:167], v[166:167], v[182:183]\n"
      "v_pk_add_f32 v[168:169], v[168:169], v[184:185]\n"
      "v_pk_add_f32 v[170:171], v[170:171], v[186:187]\n"
      "v_pk_add_f32 v[172:173], v[172:173], v[188:189]\n"
      "v_pk_add_f32 v[174:175], v[174:175], v[190:191]\n"
      "v_pk_add_f32 v[144:145], v[144:145], v[192:193]\n"
      "v_pk_add_f32 v[146:147], v[146:147], v[194:195]\n"
      "v_pk_add_f32 v[148:149], v[148:149], v[196:197]\n"
      "v_pk_add_f32 v[150:151], v[150:151], v[198:199]\n"
      "v_pk_add_f32 v[144:145], v[144:145], v[160:161]\n"
      "v_pk_add_f32 v[146:147], v[146:147], v[162:163]\n"
      "v_pk_add_f32 v[148:149], v[148:149], v[164:165]\n"
      "v_pk_add_f32 v[150:151], v[150:151], v[166:167]\n"
      "v_cvt_pk_bf16_f32 v32, v144, v145\n"
      "v_cvt_pk_bf16_f32 v34, v148, v149\n"
      "v_cvt_pk_bf16_f32 v33, v146, v147\n"
      "v_cvt_pk_bf16_f32 v35, v150, v151\n"
      "s_nop 0\n"
      "v_permlane16_swap_b32 v32, v34\n"
      "v_permlane16_swap_b32 v33, v35\n"
      "buffer_store_dwordx4 v[32:35], v6, s[68:71], 0 offen sc1\n"
      "v_pk_add_f32 v[152:153], v[152:153], v[200:201]\n"
      "v_pk_add_f32 v[154:155], v[154:155], v[202:203]\n"
      "v_pk_add_f32 v[156:157], v[156:157], v[204:205]\n"
      "v_pk_add_f32 v[158:159], v[158:159], v[206:207]\n"
      "v_pk_add_f32 v[152:153], v[152:153], v[168:169]\n"
      "v_pk_add_f32 v[154:155], v[154:155], v[170:171]\n"
      "v_pk_add_f32 v[156:157], v[156:157], v[172:173]\n"
      "v_pk_add_f32 v[158:159], v[158:159], v[174:175]\n"
      "v_cvt_pk_bf16_f32 v36, v152, v153\n"
      "v_cvt_pk_bf16_f32 v38, v156, v157\n"
      "v_cvt_pk_bf16_f32 v37, v154, v155\n"
      "v_cvt_pk_bf16_f32 v39, v158, v159\n"
      "s_nop 0\n"
      "v_permlane16_swap_b32 v36, v38\n"
      "v_permlane16_swap_b32 v37, v39\n"
      "buffer_store_dwordx4 v[36:39], v6, s[68:71], 0 offen offset:64 sc1\n"
      ".LABORT_%=:\n"
      "s_endpgm\n"
      ".LSPIN1_%=:\n"
      "s_waitcnt lgkmcnt(0)\n"
      "s_and_b32 s42, s80, s82\n"
      "s_and_b32 s42, s42, s84\n"
      "s_and_b32 s42, s42, s86\n"
      "s_and_b32 s42, s42, s88\n"
      "s_and_b32 s42, s42, s90\n"
      "s_and_b32 s42, s42, s92\n"
      "s_bitcmp0_b32 s42, 1\n"
      "s_cbranch_scc0 .LSPIN1_X_%=\n"
      "s_load_dwordx16 s[80:95], s[74:75], 0x0 glc\n"
      "s_sub_u32 s54, s54, 1\n"
      "s_cbranch_scc0 .LSPIN1_%=\n"
      "s_endpgm\n"
      ".LSPIN2_%=:\n"
      "s_waitcnt lgkmcnt(0)\n"
      "s_and_b32 s42, s80, s82\n"
      "s_and_b32 s42, s42, s84\n"
      "s_and_b32 s42, s42, s86\n"
      "s_and_b32 s42, s42, s88\n"
      "s_and_b32 s42, s42, s90\n"
      "s_and_b32 s42, s42, s92\n"
      "s_bitcmp0_b32 s42, 2\n"
      "s_cbranch_scc0 .LSPIN2_X_%=\n"
      "s_load_dwordx16 s[80:95], s[74:75], 0x0 glc\n"
      "s_sub_u32 s54, s54, 1\n"
      "s_cbranch_scc0 .LSPIN2_%=\n"
      "s_endpgm\n"
      ".p2align 12\n"
      ".LC_BUSY_%=:\n"
      "s_setprio 1\n"
      "s_add_u32 s59, s59, s34\n"
      "s_mul_i32 s42, s34, 7\n"
      "s_sub_u32 s35, s19, s42\n"
      "s_mul_i32 s67, s58, 7\n"
      "s_add_u32 s67, s67, s35\n"
      "s_lshl2_add_u32 s50, s59, s55\n"
      "s_lshl_b32 s42, s50, 5\n"
      "s_add_u32 s42, s42, s58\n"
      "s_lshl_b32 s42, s42, 7\n"
      "s_add_u32 s42, s42, 0x800000\n"
      "v_and_b32 v2, 7, v1\n"
      "v_lshl_add_u32 v198, v2, 9, s42\n"
      "s_waitcnt lgkmcnt(0)\n"
      "s_mov_b64 s[76:77], s[12:13]\n"
      "s_mov_b32 s78, 0x820000\n"
      "s_mov_b32 s79, 0x20000\n"
      "s_mov_b64 exec, 0xff\n"
      "buffer_load_dwordx2 v[200:201], v198, s[76:79], 0 offen offset:56 sc1\n"
      "s_mov_b64 exec, -1\n"
      "s_lshl_b32 s42, s35, 3\n"
      "v_add_u32 v198, s42, v198\n"
      "s_mul_i32 s42, s50, 28\n"
      "s_add_u32 s42, s42, s67\n"
      "s_lshl_b32 s42, s42, 13\n"
      "v_lshl_add_u32 v199, v1, 4, s42\n"
      "v_add_u32 v206, 0x1000, v199\n"
      "v_lshrrev_b32 v2, 4, v1\n"
      "v_bfe_u32 v3, v1, 1, 3\n"
      "v_xor_b32 v3, v3, v2\n"
      "v_lshlrev_b32 v3, 4, v3\n"
      "v_and_b32 v4, 15, v1\n"
      "v_lshl_or_b32 v3, v4, 7, v3\n"
      "s_lshl_b32 s42, s55, 13\n"
      "s_add_u32 s42, s42, 0x10000\n"
      "v_add_u32 v192, s42, v3\n"
      "v_xor_b32 v193, 64, v192\n"
      "v_bfe_u32 v3, v1, 2, 2\n"
      "v_lshl_add_u32 v3, v2, 3, v3\n"
      "v_lshlrev_b32 v3, 7, v3\n"
      "v_bfe_u32 v4, v1, 3, 2\n"
      "v_lshl_or_b32 v3, v4, 5, v3\n"
      "v_and_b32 v4, 3, v1\n"
      "v_lshl_or_b32 v194, v4, 3, v3\n"
      "v_xor_b32 v195, 32, v194\n"
      "v_xor_b32 v196, 64, v194\n"
      "v_xor_b32 v197, 96, v194\n"
      "s_mov_b32 s56, 0xbdd2d3e8\n"
      "v_mov_b32 v204, 0xc0135761\n"
      "v_mov_b64 v[0:1], 0\n"
      "v_mov_b64 v[2:3], 0\n"
      "v_mov_b64 v[4:5], 0\n"
      "v_mov_b64 v[6:7], 0\n"
      "v_mov_b64 v[8:9], 0\n"
      "v_mov_b64 v[10:11], 0\n"
      "v_mov_b64 v[12:13], 0\n"
      "v_mov_b64 v[14:15], 0\n"
      "v_mov_b64 v[16:17], 0\n"
      "v_mov_b64 v[18:19], 0\n"
      "v_mov_b64 v[20:21], 0\n"
      "v_mov_b64 v[22:23], 0\n"
      "v_mov_b64 v[24:25], 0\n"
      "v_mov_b64 v[26:27], 0\n"
      "v_mov_b64 v[28:29], 0\n"
      "v_mov_b64 v[30:31], 0\n"
      "v_mov_b64 v[32:33], 0\n"
      "v_mov_b64 v[34:35], 0\n"
      "v_mov_b64 v[36:37], 0\n"
      "v_mov_b64 v[38:39], 0\n"
      "v_mov_b64 v[40:41], 0\n"
      "v_mov_b64 v[42:43], 0\n"
      "v_mov_b64 v[44:45], 0\n"
      "v_mov_b64 v[46:47], 0\n"
      "v_mov_b64 v[48:49], 0\n"
      "v_mov_b64 v[50:51], 0\n"
      "v_mov_b64 v[52:53], 0\n"
      "v_mov_b64 v[54:55], 0\n"
      "v_mov_b64 v[56:57], 0\n"
      "v_mov_b64 v[58:59], 0\n"
      "v_mov_b64 v[60:61], 0\n"
      "v_mov_b64 v[62:63], 0\n"
      "v_mov_b64 v[64:65], 0\n"
      "v_mov_b64 v[66:67], 0\n"
      "v_mov_b64 v[68:69], 0\n"
      "v_mov_b64 v[70:71], 0\n"
      "v_mov_b64 v[72:73], 0\n"
      "v_mov_b64 v[74:75], 0\n"
      "v_mov_b64 v[76:77], 0\n"
      "v_mov_b64 v[78:79], 0\n"
      "v_mov_b64 v[80:81], 0\n"
      "v_mov_b64 v[82:83], 0\n"
      "v_mov_b64 v[84:85], 0\n"
      "v_mov_b64 v[86:87], 0\n"
      "v_mov_b64 v[88:89], 0\n"
      "v_mov_b64 v[90:91], 0\n"
      "v_mov_b64 v[92:93], 0\n"
      "v_mov_b64 v[94:95], 0\n"
      "v_mov_b64 v[96:97], 0\n"
      "v_mov_b64 v[98:99], 0\n"
      "v_mov_b64 v[100:101], 0\n"
      "v_mov_b64 v[102:103], 0\n"
      "v_mov_b64 v[104:105], 0\n"
      "v_mov_b64 v[106:107], 0\n"
      "v_mov_b64 v[108:109], 0\n"
      "v_mov_b64 v[110:111], 0\n"
      "v_mov_b64 v[112:113], 0\n"
      "v_mov_b64 v[114:115], 0\n"
      "v_mov_b64 v[116:117], 0\n"
      "v_mov_b64 v[118:119], 0\n"
      "v_mov_b64 v[120:121], 0\n"
      "v_mov_b64 v[122:123], 0\n"
      "v_mov_b64 v[124:125], 0\n"
      "v_mov_b64 v[126:127], 0\n"
      "s_mov_b32 s54, 0\n"
      "s_movk_i32 s43, 0x4000\n"
      "s_mov_b32 s44, 0xffff0000\n"
      "s_mov_b32 s45, 0x8000\n"
      "s_barrier\n"
      "ds_read_b64_tr_b16 v[160:161], v194 offset:0\n"
      "ds_read_b64_tr_b16 v[162:163], v194 offset:512\n"
      "ds_read_b64_tr_b16 v[164:165], v194 offset:4096\n"
      "ds_read_b64_tr_b16 v[166:167], v194 offset:4608\n"
      "ds_read_b64_tr_b16 v[168:169], v194 offset:8192\n"
      "ds_read_b64_tr_b16 v[170:171], v194 offset:8704\n"
      "ds_read_b64_tr_b16 v[172:173], v194 offset:12288\n"
      "ds_read_b64_tr_b16 v[174:175], v194 offset:12800\n"
      "ds_read_b128 v[128:131], v192 offset:0\n"
      "ds_read_b128 v[132:135], v193 offset:0\n"
      "ds_read_b128 v[136:139], v192 offset:2048\n"
      "ds_read_b128 v[140:143], v193 offset:2048\n"
      "ds_read_b128 v[144:147], v192 offset:4096\n"
      "ds_read_b128 v[148:151], v193 offset:4096\n"
      "ds_read_b128 v[152:155], v192 offset:6144\n"
      "ds_read_b128 v[156:159], v193 offset:6144\n"
      ".LC_LOOP_%=:\n"
      "s_waitcnt lgkmcnt(4)\n"
      "v_mfma_f32_16x16x32_bf16 v[0:3], v[160:163], v[128:131], v[0:3]\n"
      "ds_read_b64_tr_b16 v[176:177], v195 offset:0\n"
      "v_mfma_f32_16x16x32_bf16 v[0:3], v[164:167], v[132:135], v[0:3]\n"
      "ds_read_b64_tr_b16 v[178:179], v195 offset:512\n"
      "v_mfma_f32_16x16x32_bf16 v[64:67], v[168:171], v[128:131], v[64:67]\n"
      "ds_read_b64_tr_b16 v[180:181], v195 offset:4096\n"
      "v_mfma_f32_16x16x32_bf16 v[64:67], v[172:175], v[132:135], v[64:67]\n"
      "ds_read_b64_tr_b16 v[182:183], v195 offset:4608\n"
      "v_mfma_f32_16x16x32_bf16 v[68:71], v[168:171], v[136:139], v[68:71]\n"
      "ds_read_b64_tr_b16 v[184:185], v195 offset:8192\n"
      "v_mfma_f32_16x16x32_bf16 v[68:71], v[172:175], v[140:143], v[68:71]\n"
      "ds_read_b64_tr_b16 v[186:187], v195 offset:8704\n"
      "v_mfma_f32_16x16x32_bf16 v[4:7], v[160:163], v[136:139], v[4:7]\n"
      "ds_read_b64_tr_b16 v[188:189], v195 offset:12288\n"
      "v_mfma_f32_16x16x32_bf16 v[4:7], v[164:167], v[140:143], v[4:7]\n"
      "ds_read_b64_tr_b16 v[190:191], v195 offset:12800\n"
      "s_waitcnt lgkmcnt(8)\n"
      "v_mfma_f32_16x16x32_bf16 v[8:11], v[160:163], v[144:147], v[8:11]\n"
      "s_add_u32 s54, s54, 1\n"
      "v_mfma_f32_16x16x32_bf16 v[8:11], v[164:167], v[148:151], v[8:11]\n"
      "s_bitcmp1_b32 0x48, s54\n"
      "v_mfma_f32_16x16x32_bf16 v[72:75], v[168:171], v[144:147], v[72:75]\n"
      "s_cselect_b32 s40, s44, s45\n"
      "v_mfma_f32_16x16x32_bf16 v[72:75], v[172:175], v[148:151], v[72:75]\n"
      "v_add_u32 v192, s40, v192\n"
      "v_mfma_f32_16x16x32_bf16 v[76:79], v[168:171], v[152:155], v[76:79]\n"
      "v_add_u32 v193, s40, v193\n"
      "v_mfma_f32_16x16x32_bf16 v[76:79], v[172:175], v[156:159], v[76:79]\n"
      "v_xor_b32 v194, s43, v194\n"
      "v_mfma_f32_16x16x32_bf16 v[12:15], v[160:163], v[152:155], v[12:15]\n"
      "v_xor_b32 v195, s43, v195\n"
      "v_mfma_f32_16x16x32_bf16 v[12:15], v[164:167], v[156:159], v[12:15]\n"
      "s_waitcnt lgkmcnt(0)\n"
      "v_mfma_f32_16x16x32_bf16 v[16:19], v[176:179], v[128:131], v[16:19]\n"
      "ds_read_b64_tr_b16 v[160:161], v196 offset:0\n"
      "v_mfma_f32_16x16x32_bf16 v[16:19], v[180:183], v[132:135], v[16:19]\n"
      "ds_read_b64_tr_b16 v[162:163], v196 offset:512\n"
      "v_mfma_f32_16x16x32_bf16 v[20:23], v[176:179], v[136:139], v[20:23]\n"
      "ds_read_b64_tr_b16 v[164:165], v196 offset:4096\n"
      "v_mfma_f32_16x16x32_bf16 v[20:23], v[180:183], v[140:143], v[20:23]\n"
      "ds_read_b64_tr_b16 v[166:167], v196 offset:4608\n"
      "v_mfma_f32_16x16x32_bf16 v[24:27], v[176:179], v[144:147], v[24:27]\n"
      "ds_read_b64_tr_b16 v[168:169], v196 offset:8192\n"
      "v_mfma_f32_16x16x32_bf16 v[24:27], v[180:183], v[148:151], v[24:27]\n"
      "ds_read_b64_tr_b16 v[170:171], v196 offset:8704\n"
      "v_mfma_f32_16x16x32_bf16 v[28:31], v[176:179], v[152:155], v[28:31]\n"
      "ds_read_b64_tr_b16 v[172:173], v196 offset:12288\n"
      "v_mfma_f32_16x16x32_bf16 v[28:31], v[180:183], v[156:159], v[28:31]\n"
      "ds_read_b64_tr_b16 v[174:175], v196 offset:12800\n"
      "v_mfma_f32_16x16x32_bf16 v[92:95], v[184:187], v[152:155], v[92:95]\n"
      "v_xor_b32 v196, s43, v196\n"
      "v_mfma_f32_16x16x32_bf16 v[92:95], v[188:191], v[156:159], v[92:95]\n"
      "v_mfma_f32_16x16x32_bf16 v[88:91], v[184:187], v[144:147], v[88:91]\n"
      "v_mfma_f32_16x16x32_bf16 v[88:91], v[188:191], v[148:151], v[88:91]\n"
      "v_mfma_f32_16x16x32_bf16 v[84:87], v[184:187], v[136:139], v[84:87]\n"
      "v_mfma_f32_16x16x32_bf16 v[84:87], v[188:191], v[140:143], v[84:87]\n"
      "v_mfma_f32_16x16x32_bf16 v[80:83], v[184:187], v[128:131], v[80:83]\n"
      "v_mfma_f32_16x16x32_bf16 v[80:83], v[188:191], v[132:135], v[80:83]\n"
      "s_waitcnt lgkmcnt(0)\n"
      "v_mfma_f32_16x16x32_bf16 v[32:35], v[160:163], v[128:131], v[32:35]\n"
      "ds_read_b64_tr_b16 v[176:177], v197 offset:0\n"
      "v_mfma_f32_16x16x32_bf16 v[32:35], v[164:167], v[132:135], v[32:35]\n"
      "ds_read_b64_tr_b16 v[178:179], v197 offset:512\n"
      "v_mfma_f32_16x16x32_bf16 v[36:39], v[160:163], v[136:139], v[36:39]\n"
      "ds_read_b64_tr_b16 v[180:181], v197 offset:4096\n"
      "v_mfma_f32_16x16x32_bf16 v[36:39], v[164:167], v[140:143], v[36:39]\n"
      "ds_read_b64_tr_b16 v[182:183], v197 offset:4608\n"
      "v_mfma_f32_16x16x32_bf16 v[40:43], v[160:163], v[144:147], v[40:43]\n"
      "ds_read_b64_tr_b16 v[184:185], v197 offset:8192\n"
      "v_mfma_f32_16x16x32_bf16 v[40:43], v[164:167], v[148:151], v[40:43]\n"
      "ds_read_b64_tr_b16 v[186:187], v197 offset:8704\n"
      "v_mfma_f32_16x16x32_bf16 v[44:47], v[160:163], v[152:155], v[44:47]\n"
      "ds_read_b64_tr_b16 v[188:189], v197 offset:12288\n"
      "v_mfma_f32_16x16x32_bf16 v[44:47], v[164:167], v[156:159], v[44:47]\n"
      "ds_read_b64_tr_b16 v[190:191], v197 offset:12800\n"
      "v_mfma_f32_16x16x32_bf16 v[108:111], v[168:171], v[152:155], v[108:111]\n"
      "v_xor_b32 v197, s43, v197\n"
      "v_mfma_f32_16x16x32_bf16 v[108:111], v[172:175], v[156:159], v[108:111]\n"
      "s_cmp_lt_u32 s54, 8\n"
      "v_mfma_f32_16x16x32_bf16 v[104:107], v[168:171], v[144:147], v[104:107]\n"
      "v_mfma_f32_16x16x32_bf16 v[104:107], v[172:175], v[148:151], v[104:107]\n"
      "v_mfma_f32_16x16x32_bf16 v[100:103], v[168:171], v[136:139], v[100:103]\n"
      "v_mfma_f32_16x16x32_bf16 v[100:103], v[172:175], v[140:143], v[100:103]\n"
      "v_mfma_f32_16x16x32_bf16 v[96:99], v[168:171], v[128:131], v[96:99]\n"
      "v_mfma_f32_16x16x32_bf16 v[96:99], v[172:175], v[132:135], v[96:99]\n"
      "s_waitcnt lgkmcnt(0)\n"
      "s_cbranch_scc0 .LC_NOBAR_%=\n"
      "s_barrier\n"
      ".LC_NOBAR_%=:\n"
      "v_mfma_f32_16x16x32_bf16 v[48:51], v[176:179], v[128:131], v[48:51]\n"
      "ds_read_b64_tr_b16 v[160:161], v194 offset:0\n"
      "v_mfma_f32_16x16x32_bf16 v[48:51], v[180:183], v[132:135], v[48:51]\n"
      "ds_read_b64_tr_b16 v[162:163], v194 offset:512\n"
      "v_mfma_f32_16x16x32_bf16 v[112:115], v[184:187], v[128:131], v[112:115]\n"
      "ds_read_b64_tr_b16 v[164:165], v194 offset:4096\n"
      "v_mfma_f32_16x16x32_bf16 v[112:115], v[188:191], v[132:135], v[112:115]\n"
      "ds_read_b64_tr_b16 v[166:167], v194 offset:4608\n"
      "v_mfma_f32_16x16x32_bf16 v[116:119], v[184:187], v[136:139], v[116:119]\n"
      "ds_read_b128 v[128:131], v192 offset:0\n"
      "v_mfma_f32_16x16x32_bf16 v[116:119], v[188:191], v[140:143], v[116:119]\n"
      "ds_read_b128 v[132:135], v193 offset:0\n"
      "v_mfma_f32_16x16x32_bf16 v[52:55], v[176:179], v[136:139], v[52:55]\n"
      "ds_read_b64_tr_b16 v[168:169], v194 offset:8192\n"
      "v_mfma_f32_16x16x32_bf16 v[52:55], v[180:183], v[140:143], v[52:55]\n"
      "ds_read_b64_tr_b16 v[170:171], v194 offset:8704\n"
      "v_mfma_f32_16x16x32_bf16 v[56:59], v[176:179], v[144:147], v[56:59]\n"
      "ds_read_b128 v[136:139], v192 offset:2048\n"
      "v_mfma_f32_16x16x32_bf16 v[56:59], v[180:183], v[148:151], v[56:59]\n"
      "ds_read_b128 v[140:143], v193 offset:2048\n"
      "v_mfma_f32_16x16x32_bf16 v[120:123], v[184:187], v[144:147], v[120:123]\n"
      "ds_read_b64_tr_b16 v[172:173], v194 offset:12288\n"
      "v_mfma_f32_16x16x32_bf16 v[120:123], v[188:191], v[148:151], v[120:123]\n"
      "ds_read_b64_tr_b16 v[174:175], v194 offset:12800\n"
      "v_mfma_f32_16x16x32_bf16 v[124:127], v[184:187], v[152:155], v[124:127]\n"
      "ds_read_b128 v[144:147], v192 offset:4096\n"
      "v_mfma_f32_16x16x32_bf16 v[124:127], v[188:191], v[156:159], v[124:127]\n"
      "ds_read_b128 v[148:151], v193 offset:4096\n"
      "v_mfma_f32_16x16x32_bf16 v[60:63], v[176:179], v[152:155], v[60:63]\n"
      "ds_read_b128 v[152:155], v192 offset:6144\n"
      "v_mfma_f32_16x16x32_bf16 v[60:63], v[180:183], v[156:159], v[60:63]\n"
      "ds_read_b128 v[156:159], v193 offset:6144\n"
      "s_cbranch_scc1 .LC_LOOP_%=\n"
      "v_pk_mul_f32 v[208:209], v[0:1], v[0:1]\n"
      "v_pk_mul_f32 v[210:211], v[2:3], v[2:3]\n"
      "v_pk_mul_f32 v[212:213], v[16:17], v[16:17]\n"
      "v_pk_mul_f32 v[214:215], v[18:19], v[18:19]\n"
      "v_pk_fma_f32 v[208:209], v[208:209], s[56:57], v[204:205] op_sel_hi:[1,0,0]\n"
      "v_pk_fma_f32 v[210:211], v[210:211], s[56:57], v[204:205] op_sel_hi:[1,0,0]\n"
      "v_pk_fma_f32 v[212:213], v[212:213], s[56:57], v[204:205] op_sel_hi:[1,0,0]\n"
      "v_pk_fma_f32 v[214:215], v[214:215], s[56:57], v[204:205] op_sel_hi:[1,0,0]\n"
      "v_pk_mul_f32 v[208:209], v[208:209], v[0:1]\n"
      "v_pk_mul_f32 v[210:211], v[210:211], v[2:3]\n"
      "v_pk_mul_f32 v[212:213], v[212:213], v[16:17]\n"
      "v_pk_mul_f32 v[214:215], v[214:215], v[18:19]\n"
      "v_exp_f32 v208, v208\n"
      "v_exp_f32 v209, v209\n"
      "v_exp_f32 v210, v210\n"
      "v_exp_f32 v211, v211\n"
      "v_exp_f32 v212, v212\n"
      "v_exp_f32 v213, v213\n"
      "v_exp_f32 v214, v214\n"
      "v_exp_f32 v215, v215\n"
      "v_pk_mul_f32 v[64:65], v[64:65], v[0:1]\n"
      "v_pk_mul_f32 v[66:67], v[66:67], v[2:3]\n"
      "v_pk_mul_f32 v[80:81], v[80:81], v[16:17]\n"
      "v_pk_mul_f32 v[82:83], v[82:83], v[18:19]\n"
      "v_pk_add_f32 v[208:209], v[208:209], 1.0 op_sel_hi:[1,0]\n"
      "v_pk_add_f32 v[210:211], v[210:211], 1.0 op_sel_hi:[1,0]\n"
      "v_pk_add_f32 v[212:213], v[212:213], 1.0 op_sel_hi:[1,0]\n"
      "v_pk_add_f32 v[214:215], v[214:215], 1.0 op_sel_hi:[1,0]\n"
      "v_rcp_f32 v208, v208\n"
      "v_rcp_f32 v209, v209\n"
      "v_rcp_f32 v210, v210\n"
      "v_rcp_f32 v211, v211\n"
      "v_rcp_f32 v212, v212\n"
      "v_rcp_f32 v213, v213\n"
      "v_rcp_f32 v214, v214\n"
      "v_rcp_f32 v215, v215\n"
      "v_pk_mul_f32 v[64:65], v[64:65], v[208:209]\n"
      "v_pk_mul_f32 v[66:67], v[66:67], v[210:211]\n"
      "v_pk_mul_f32 v[80:81], v[80:81], v[212:213]\n"
      "v_pk_mul_f32 v[82:83], v[82:83], v[214:215]\n"
      "v_cvt_pk_bf16_f32 v216, v64, v65\n"
      "v_cvt_pk_bf16_f32 v217, v66, v67\n"
      "v_cvt_pk_bf16_f32 v218, v80, v81\n"
      "v_cvt_pk_bf16_f32 v219, v82, v83\n"
      "buffer_store_dwordx4 v[216:219], v199, s[76:79], 0 offen sc1\n"
      "v_pk_mul_f32 v[208:209], v[4:5], v[4:5]\n"
      "v_pk_mul_f32 v[210:211], v[6:7], v[6:7]\n"
      "v_pk_mul_f32 v[212:213], v[20:21], v[20:21]\n"
      "v_pk_mul_f32 v[214:215], v[22:23], v[22:23]\n"
      "v_pk_fma_f32 v[208:209], v[208:209], s[56:57], v[204:205] op_sel_hi:[1,0,0]\n"
      "v_pk_fma_f32 v[210:211], v[210:211], s[56:57], v[204:205] op_sel_hi:[1,0,0]\n"
      "v_pk_fma_f32 v[212:213], v[212:213], s[56:57], v[204:205] op_sel_hi:[1,0,0]\n"
      "v_pk_fma_f32 v[214:215], v[214:215], s[56:57], v[204:205] op_sel_hi:[1,0,0]\n"
      "v_pk_mul_f32 v[208:209], v[208:209], v[4:5]\n"
      "v_pk_mul_f32 v[210:211], v[210:211], v[6:7]\n"
      "v_pk_mul_f32 v[212:213], v[212:213], v[20:21]\n"
      "v_pk_mul_f32 v[214:215], v[214:215], v[22:23]\n"
      "v_exp_f32 v208, v208\n"
      "v_exp_f32 v209, v209\n"
      "v_exp_f32 v210, v210\n"
      "v_exp_f32 v211, v211\n"
      "v_exp_f32 v212, v212\n"
      "v_exp_f32 v213, v213\n"
      "v_exp_f32 v214, v214\n"
      "v_exp_f32 v215, v215\n"
      "v_pk_mul_f32 v[68:69], v[68:69], v[4:5]\n"
      "v_pk_mul_f32 v[70:71], v[70:71], v[6:7]\n"
      "v_pk_mul_f32 v[84:85], v[84:85], v[20:21]\n"
      "v_pk_mul_f32 v[86:87], v[86:87], v[22:23]\n"
      "v_pk_add_f32 v[208:209], v[208:209], 1.0 op_sel_hi:[1,0]\n"
      "v_pk_add_f32 v[210:211], v[210:211], 1.0 op_sel_hi:[1,0]\n"
      "v_pk_add_f32 v[212:213], v[212:213], 1.0 op_sel_hi:[1,0]\n"
      "v_pk_add_f32 v[214:215], v[214:215], 1.0 op_sel_hi:[1,0]\n"
      "v_rcp_f32 v208, v208\n"
      "v_rcp_f32 v209, v209\n"
      "v_rcp_f32 v210, v210\n"
      "v_rcp_f32 v211, v211\n"
      "v_rcp_f32 v212, v212\n"
      "v_rcp_f32 v213, v213\n"
      "v_rcp_f32 v214, v214\n"
      "v_rcp_f32 v215, v215\n"
      "v_pk_mul_f32 v[68:69], v[68:69], v[208:209]\n"
      "v_pk_mul_f32 v[70:71], v[70:71], v[210:211]\n"
      "v_pk_mul_f32 v[84:85], v[84:85], v[212:213]\n"
      "v_pk_mul_f32 v[86:87], v[86:87], v[214:215]\n"
      "v_cvt_pk_bf16_f32 v220, v68, v69\n"
      "v_cvt_pk_bf16_f32 v221, v70, v71\n"
      "v_cvt_pk_bf16_f32 v222, v84, v85\n"
      "v_cvt_pk_bf16_f32 v223, v86, v87\n"
      "buffer_store_dwordx4 v[220:223], v199, s[76:79], 0 offen offset:1024 sc1\n"
      "v_pk_mul_f32 v[208:209], v[8:9], v[8:9]\n"
      "v_pk_mul_f32 v[210:211], v[10:11], v[10:11]\n"
      "v_pk_mul_f32 v[212:213], v[24:25], v[24:25]\n"
      "v_pk_mul_f32 v[214:215], v[26:27], v[26:27]\n"
      "v_pk_fma_f32 v[208:209], v[208:209], s[56:57], v[204:205] op_sel_hi:[1,0,0]\n"
      "v_pk_fma_f32 v[210:211], v[210:211], s[56:57], v[204:205] op_sel_hi:[1,0,0]\n"
      "v_pk_fma_f32 v[212:213], v[212:213], s[56:57], v[204:205] op_sel_hi:[1,0,0]\n"
      "v_pk_fma_f32 v[214:215], v[214:215], s[56:57], v[204:205] op_sel_hi:[1,0,0]\n"
      "v_pk_mul_f32 v[208:209], v[208:209], v[8:9]\n"
      "v_pk_mul_f32 v[210:211], v[210:211], v[10:11]\n"
      "v_pk_mul_f32 v[212:213], v[212:213], v[24:25]\n"
      "v_pk_mul_f32 v[214:215], v[214:215], v[26:27]\n"
      "v_exp_f32 v208, v208\n"
      "v_exp_f32 v209, v209\n"
      "v_exp_f32 v210, v210\n"
      "v_exp_f32 v211, v211\n"
      "v_exp_f32 v212, v212\n"
      "v_exp_f32 v213, v213\n"
      "v_exp_f32 v214, v214\n"
      "v_exp_f32 v215, v215\n"
      "v_pk_mul_f32 v[72:73], v[72:73], v[8:9]\n"
      "v_pk_mul_f32 v[74:75], v[74:75], v[10:11]\n"
      "v_pk_mul_f32 v[88:89], v[88:89], v[24:25]\n"
      "v_pk_mul_f32 v[90:91], v[90:91], v[26:27]\n"
      "v_pk_add_f32 v[208:209], v[208:209], 1.0 op_sel_hi:[1,0]\n"
      "v_pk_add_f32 v[210:211], v[210:211], 1.0 op_sel_hi:[1,0]\n"
      "v_pk_add_f32 v[212:213], v[212:213], 1.0 op_sel_hi:[1,0]\n"
      "v_pk_add_f32 v[214:215], v[214:215], 1.0 op_sel_hi:[1,0]\n"
      "v_rcp_f32 v208, v208\n"
      "v_rcp_f32 v209, v209\n"
      "v_rcp_f32 v210, v210\n"
      "v_rcp_f32 v211, v211\n"
      "v_rcp_f32 v212, v212\n"
      "v_rcp_f32 v213, v213\n"
      "v_rcp_f32 v214, v214\n"
      "v_rcp_f32 v215, v215\n"
      "v_pk_mul_f32 v[72:73], v[72:73], v[208:209]\n"
      "v_pk_mul_f32 v[74:75], v[74:75], v[210:211]\n"
      "v_pk_mul_f32 v[88:89], v[88:89], v[212:213]\n"
      "v_pk_mul_f32 v[90:91], v[90:91], v[214:215]\n"
      "v_cvt_pk_bf16_f32 v224, v72, v73\n"
      "v_cvt_pk_bf16_f32 v225, v74, v75\n"
      "v_cvt_pk_bf16_f32 v226, v88, v89\n"
      "v_cvt_pk_bf16_f32 v227, v90, v91\n"
      "buffer_store_dwordx4 v[224:227], v199, s[76:79], 0 offen offset:2048 sc1\n"
      "v_pk_mul_f32 v[208:209], v[12:13], v[12:13]\n"
      "v_pk_mul_f32 v[210:211], v[14:15], v[14:15]\n"
      "v_pk_mul_f32 v[212:213], v[28:29], v[28:29]\n"
      "v_pk_mul_f32 v[214:215], v[30:31], v[30:31]\n"
      "v_pk_fma_f32 v[208:209], v[208:209], s[56:57], v[204:205] op_sel_hi:[1,0,0]\n"
      "v_pk_fma_f32 v[210:211], v[210:211], s[56:57], v[204:205] op_sel_hi:[1,0,0]\n"
      "v_pk_fma_f32 v[212:213], v[212:213], s[56:57], v[204:205] op_sel_hi:[1,0,0]\n"
      "v_pk_fma_f32 v[214:215], v[214:215], s[56:57], v[204:205] op_sel_hi:[1,0,0]\n"
      "v_pk_mul_f32 v[208:209], v[208:209], v[12:13]\n"
      "v_pk_mul_f32 v[210:211], v[210:211], v[14:15]\n"
      "v_pk_mul_f32 v[212:213], v[212:213], v[28:29]\n"
      "v_pk_mul_f32 v[214:215], v[214:215], v[30:31]\n"
      "v_exp_f32 v208, v208\n"
      "v_exp_f32 v209, v209\n"
      "v_exp_f32 v210, v210\n"
      "v_exp_f32 v211, v211\n"
      "v_exp_f32 v212, v212\n"
      "v_exp_f32 v213, v213\n"
      "v_exp_f32 v214, v214\n"
      "v_exp_f32 v215, v215\n"
      "v_pk_mul_f32 v[76:77], v[76:77], v[12:13]\n"
      "v_pk_mul_f32 v[78:79], v[78:79], v[14:15]\n"
      "v_pk_mul_f32 v[92:93], v[92:93], v[28:29]\n"
      "v_pk_mul_f32 v[94:95], v[94:95], v[30:31]\n"
      "v_pk_add_f32 v[208:209], v[208:209], 1.0 op_sel_hi:[1,0]\n"
      "v_pk_add_f32 v[210:211], v[210:211], 1.0 op_sel_hi:[1,0]\n"
      "v_pk_add_f32 v[212:213], v[212:213], 1.0 op_sel_hi:[1,0]\n"
      "v_pk_add_f32 v[214:215], v[214:215], 1.0 op_sel_hi:[1,0]\n"
      "v_rcp_f32 v208, v208\n"
      "v_rcp_f32 v209, v209\n"
      "v_rcp_f32 v210, v210\n"
      "v_rcp_f32 v211, v211\n"
      "v_rcp_f32 v212, v212\n"
      "v_rcp_f32 v213, v213\n"
      "v_rcp_f32 v214, v214\n"
      "v_rcp_f32 v215, v215\n"
      "v_pk_mul_f32 v[76:77], v[76:77], v[208:209]\n"
      "v_pk_mul_f32 v[78:79], v[78:79], v[210:211]\n"
      "v_pk_mul_f32 v[92:93], v[92:93], v[212:213]\n"
      "v_pk_mul_f32 v[94:95], v[94:95], v[214:215]\n"
      "v_cvt_pk_bf16_f32 v228, v76, v77\n"
      "v_cvt_pk_bf16_f32 v229, v78, v79\n"
      "v_cvt_pk_bf16_f32 v230, v92, v93\n"
      "v_cvt_pk_bf16_f32 v231, v94, v95\n"
      "buffer_store_dwordx4 v[228:231], v199, s[76:79], 0 offen offset:3072 sc1\n"
      "v_pk_mul_f32 v[208:209], v[36:37], v[36:37]\n"
      "v_pk_mul_f32 v[210:211], v[38:39], v[38:39]\n"
      "v_pk_mul_f32 v[212:213], v[52:53], v[52:53]\n"
      "v_pk_mul_f32 v[214:215], v[54:55], v[54:55]\n"
      "v_pk_fma_f32 v[208:209], v[208:209], s[56:57], v[204:205] op_sel_hi:[1,0,0]\n"
      "v_pk_fma_f32 v[210:211], v[210:211], s[56:57], v[204:205] op_sel_hi:[1,0,0]\n"
      "v_pk_fma_f32 v[212:213], v[212:213], s[56:57], v[204:205] op_sel_hi:[1,0,0]\n"
      "v_pk_fma_f32 v[214:215], v[214:215], s[56:57], v[204:205] op_sel_hi:[1,0,0]\n"
      "v_pk_mul_f32 v[208:209], v[208:209], v[36:37]\n"
      "v_pk_mul_f32 v[210:211], v[210:211], v[38:39]\n"
      "v_pk_mul_f32 v[212:213], v[212:213], v[52:53]\n"
      "v_pk_mul_f32 v[214:215], v[214:215], v[54:55]\n"
      "v_exp_f32 v208, v208\n"
      "v_exp_f32 v209, v209\n"
      "v_exp_f32 v210, v210\n"
      "v_exp_f32 v211, v211\n"
      "v_exp_f32 v212, v212\n"
      "v_exp_f32 v213, v213\n"
      "v_exp_f32 v214, v214\n"
      "v_exp_f32 v215, v215\n"
      "v_pk_mul_f32 v[100:101], v[100:101], v[36:37]\n"
      "v_pk_mul_f32 v[102:103], v[102:103], v[38:39]\n"
      "v_pk_mul_f32 v[116:117], v[116:117], v[52:53]\n"
      "v_pk_mul_f32 v[118:119], v[118:119], v[54:55]\n"
      "v_pk_add_f32 v[208:209], v[208:209], 1.0 op_sel_hi:[1,0]\n"
      "v_pk_add_f32 v[210:211], v[210:211], 1.0 op_sel_hi:[1,0]\n"
      "v_pk_add_f32 v[212:213], v[212:213], 1.0 op_sel_hi:[1,0]\n"
      "v_pk_add_f32 v[214:215], v[214:215], 1.0 op_sel_hi:[1,0]\n"
      "v_rcp_f32 v208, v208\n"
      "v_rcp_f32 v209, v209\n"
      "v_rcp_f32 v210, v210\n"
      "v_rcp_f32 v211, v211\n"
      "v_rcp_f32 v212, v212\n"
      "v_rcp_f32 v213, v213\n"
      "v_rcp_f32 v214, v214\n"
      "v_rcp_f32 v215, v215\n"
      "v_pk_mul_f32 v[100:101], v[100:101], v[208:209]\n"
      "v_pk_mul_f32 v[102:103], v[102:103], v[210:211]\n"
      "v_pk_mul_f32 v[116:117], v[116:117], v[212:213]\n"
      "v_pk_mul_f32 v[118:119], v[118:119], v[214:215]\n"
      "v_cvt_pk_bf16_f32 v232, v100, v101\n"
      "v_cvt_pk_bf16_f32 v233, v102, v103\n"
      "v_cvt_pk_bf16_f32 v234, v116, v117\n"
      "v_cvt_pk_bf16_f32 v235, v118, v119\n"
      "buffer_store_dwordx4 v[232:235], v206, s[76:79], 0 offen offset:1024 sc1\n"
      "s_waitcnt vmcnt(5)\n"
      "v_add_co_u32 v200, vcc, 0x7f4a7c15, v200\n"
      "v_mov_b32 v202, 0x9e3779b9\n"
      "v_and_b32 v200, 0xffffff00, v200\n"
      "v_addc_co_u32 v201, vcc, v201, v202, vcc\n"
      "v_or_b32 v200, 1, v200\n"
      "s_mov_b64 exec, 0xff\n"
      "s_waitcnt vmcnt(3)\n"
      "buffer_store_dwordx2 v[200:201], v198, s[76:79], 0 offen sc1\n"
      "s_mov_b64 exec, -1\n"
      "v_pk_mul_f32 v[208:209], v[40:41], v[40:41]\n"
      "v_pk_mul_f32 v[210:211], v[42:43], v[42:43]\n"
      "v_pk_mul_f32 v[212:213], v[56:57], v[56:57]\n"
      "v_pk_mul_f32 v[214:215], v[58:59], v[58:59]\n"
      "v_pk_fma_f32 v[208:209], v[208:209], s[56:57], v[204:205] op_sel_hi:[1,0,0]\n"
      "v_pk_fma_f32 v[210:211], v[210:211], s[56:57], v[204:205] op_sel_hi:[1,0,0]\n"
      "v_pk_fma_f32 v[212:213], v[212:213], s[56:57], v[204:205] op_sel_hi:[1,0,0]\n"
      "v_pk_fma_f32 v[214:215], v[214:215], s[56:57], v[204:205] op_sel_hi:[1,0,0]\n"
      "v_pk_mul_f32 v[208:209], v[208:209], v[40:41]\n"
      "v_pk_mul_f32 v[210:211], v[210:211], v[42:43]\n"
      "v_pk_mul_f32 v[212:213], v[212:213], v[56:57]\n"
      "v_pk_mul_f32 v[214:215], v[214:215], v[58:59]\n"
      "v_exp_f32 v208, v208\n"
      "v_exp_f32 v209, v209\n"
      "v_exp_f32 v210, v210\n"
      "v_exp_f32 v211, v211\n"
      "v_exp_f32 v212, v212\n"
      "v_exp_f32 v213, v213\n"
      "v_exp_f32 v214, v214\n"
      "v_exp_f32 v215, v215\n"
      "v_pk_mul_f32 v[104:105], v[104:105], v[40:41]\n"
      "v_pk_mul_f32 v[106:107], v[106:107], v[42:43]\n"
      "v_pk_mul_f32 v[120:121], v[120:121], v[56:57]\n"
      "v_pk_mul_f32 v[122:123], v[122:123], v[58:59]\n"
      "v_pk_add_f32 v[208:209], v[208:209], 1.0 op_sel_hi:[1,0]\n"
      "v_pk_add_f32 v[210:211], v[210:211], 1.0 op_sel_hi:[1,0]\n"
      "v_pk_add_f32 v[212:213], v[212:213], 1.0 op_sel_hi:[1,0]\n"
      "v_pk_add_f32 v[214:215], v[214:215], 1.0 op_sel_hi:[1,0]\n"
      "v_rcp_f32 v208, v208\n"
      "v_rcp_f32 v209, v209\n"
      "v_rcp_f32 v210, v210\n"
      "v_rcp_f32 v211, v211\n"
      "v_rcp_f32 v212, v212\n"
      "v_rcp_f32 v213, v213\n"
      "v_rcp_f32 v214, v214\n"
      "v_rcp_f32 v215, v215\n"
      "v_pk_mul_f32 v[104:105], v[104:105], v[208:209]\n"
      "v_pk_mul_f32 v[106:107], v[106:107], v[210:211]\n"
      "v_pk_mul_f32 v[120:121], v[120:121], v[212:213]\n"
      "v_pk_mul_f32 v[122:123], v[122:123], v[214:215]\n"
      "v_cvt_pk_bf16_f32 v236, v104, v105\n"
      "v_cvt_pk_bf16_f32 v237, v106, v107\n"
      "v_cvt_pk_bf16_f32 v238, v120, v121\n"
      "v_cvt_pk_bf16_f32 v239, v122, v123\n"
      "buffer_store_dwordx4 v[236:239], v206, s[76:79], 0 offen offset:2048 sc1\n"
      "v_pk_mul_f32 v[208:209], v[44:45], v[44:45]\n"
      "v_pk_mul_f32 v[210:211], v[46:47], v[46:47]\n"
      "v_pk_mul_f32 v[212:213], v[60:61], v[60:61]\n"
      "v_pk_mul_f32 v[214:215], v[62:63], v[62:63]\n"
      "v_pk_fma_f32 v[208:209], v[208:209], s[56:57], v[204:205] op_sel_hi:[1,0,0]\n"
      "v_pk_fma_f32 v[210:211], v[210:211], s[56:57], v[204:205] op_sel_hi:[1,0,0]\n"
      "v_pk_fma_f32 v[212:213], v[212:213], s[56:57], v[204:205] op_sel_hi:[1,0,0]\n"
      "v_pk_fma_f32 v[214:215], v[214:215], s[56:57], v[204:205] op_sel_hi:[1,0,0]\n"
      "v_pk_mul_f32 v[208:209], v[208:209], v[44:45]\n"
      "v_pk_mul_f32 v[210:211], v[210:211], v[46:47]\n"
      "v_pk_mul_f32 v[212:213], v[212:213], v[60:61]\n"
      "v_pk_mul_f32 v[214:215], v[214:215], v[62:63]\n"
      "v_exp_f32 v208, v208\n"
      "v_exp_f32 v209, v209\n"
      "v_exp_f32 v210, v210\n"
      "v_exp_f32 v211, v211\n"
      "v_exp_f32 v212, v212\n"
      "v_exp_f32 v213, v213\n"
      "v_exp_f32 v214, v214\n"
      "v_exp_f32 v215, v215\n"
      "v_pk_mul_f32 v[108:109], v[108:109], v[44:45]\n"
      "v_pk_mul_f32 v[110:111], v[110:111], v[46:47]\n"
      "v_pk_mul_f32 v[124:125], v[124:125], v[60:61]\n"
      "v_pk_mul_f32 v[126:127], v[126:127], v[62:63]\n"
      "v_pk_add_f32 v[208:209], v[208:209], 1.0 op_sel_hi:[1,0]\n"
      "v_pk_add_f32 v[210:211], v[210:211], 1.0 op_sel_hi:[1,0]\n"
      "v_pk_add_f32 v[212:213], v[212:213], 1.0 op_sel_hi:[1,0]\n"
      "v_pk_add_f32 v[214:215], v[214:215], 1.0 op_sel_hi:[1,0]\n"
      "v_rcp_f32 v208, v208\n"
      "v_rcp_f32 v209, v209\n"
      "v_rcp_f32 v210, v210\n"
      "v_rcp_f32 v211, v211\n"
      "v_rcp_f32 v212, v212\n"
      "v_rcp_f32 v213, v213\n"
      "v_rcp_f32 v214, v214\n"
      "v_rcp_f32 v215, v215\n"
      "v_pk_mul_f32 v[108:109], v[108:109], v[208:209]\n"
      "v_pk_mul_f32 v[110:111], v[110:111], v[210:211]\n"
      "v_pk_mul_f32 v[124:125], v[124:125], v[212:213]\n"
      "v_pk_mul_f32 v[126:127], v[126:127], v[214:215]\n"
      "v_cvt_pk_bf16_f32 v240, v108, v109\n"
      "v_cvt_pk_bf16_f32 v241, v110, v111\n"
      "v_cvt_pk_bf16_f32 v242, v124, v125\n"
      "v_cvt_pk_bf16_f32 v243, v126, v127\n"
      "buffer_store_dwordx4 v[240:243], v206, s[76:79], 0 offen offset:3072 sc1\n"
      "v_pk_mul_f32 v[208:209], v[32:33], v[32:33]\n"
      "v_pk_mul_f32 v[210:211], v[34:35], v[34:35]\n"
      "v_pk_mul_f32 v[212:213], v[48:49], v[48:49]\n"
      "v_pk_mul_f32 v[214:215], v[50:51], v[50:51]\n"
      "v_pk_fma_f32 v[208:209], v[208:209], s[56:57], v[204:205] op_sel_hi:[1,0,0]\n"
      "v_pk_fma_f32 v[210:211], v[210:211], s[56:57], v[204:205] op_sel_hi:[1,0,0]\n"
      "v_pk_fma_f32 v[212:213], v[212:213], s[56:57], v[204:205] op_sel_hi:[1,0,0]\n"
      "v_pk_fma_f32 v[214:215], v[214:215], s[56:57], v[204:205] op_sel_hi:[1,0,0]\n"
      "v_pk_mul_f32 v[208:209], v[208:209], v[32:33]\n"
      "v_pk_mul_f32 v[210:211], v[210:211], v[34:35]\n"
      "v_pk_mul_f32 v[212:213], v[212:213], v[48:49]\n"
      "v_pk_mul_f32 v[214:215], v[214:215], v[50:51]\n"
      "v_exp_f32 v208, v208\n"
      "v_exp_f32 v209, v209\n"
      "v_exp_f32 v210, v210\n"
      "v_exp_f32 v211, v211\n"
      "v_exp_f32 v212, v212\n"
      "v_exp_f32 v213, v213\n"
      "v_exp_f32 v214, v214\n"
      "v_exp_f32 v215, v215\n"
      "v_pk_mul_f32 v[96:97], v[96:97], v[32:33]\n"
      "v_pk_mul_f32 v[98:99], v[98:99], v[34:35]\n"
      "v_pk_mul_f32 v[112:113], v[112:113], v[48:49]\n"
      "v_pk_mul_f32 v[114:115], v[114:115], v[50:51]\n"
      "v_pk_add_f32 v[208:209], v[208:209], 1.0 op_sel_hi:[1,0]\n"
      "v_pk_add_f32 v[210:211], v[210:211], 1.0 op_sel_hi:[1,0]\n"
      "v_pk_add_f32 v[212:213], v[212:213], 1.0 op_sel_hi:[1,0]\n"
      "v_pk_add_f32 v[214:215], v[214:215], 1.0 op_sel_hi:[1,0]\n"
      "v_rcp_f32 v208, v208\n"
      "v_rcp_f32 v209, v209\n"
      "v_rcp_f32 v210, v210\n"
      "v_rcp_f32 v211, v211\n"
      "v_rcp_f32 v212, v212\n"
      "v_rcp_f32 v213, v213\n"
      "v_rcp_f32 v214, v214\n"
      "v_rcp_f32 v215, v215\n"
      "v_pk_mul_f32 v[96:97], v[96:97], v[208:209]\n"
      "v_pk_mul_f32 v[98:99], v[98:99], v[210:211]\n"
      "v_pk_mul_f32 v[112:113], v[112:113], v[212:213]\n"
      "v_pk_mul_f32 v[114:115], v[114:115], v[214:215]\n"
      "v_cvt_pk_bf16_f32 v244, v96, v97\n"
      "v_cvt_pk_bf16_f32 v245, v98, v99\n"
      "v_cvt_pk_bf16_f32 v246, v112, v113\n"
      "v_cvt_pk_bf16_f32 v247, v114, v115\n"
      "buffer_store_dwordx4 v[244:247], v206, s[76:79], 0 offen sc1\n"
      "s_barrier\n"
      "v_or_b32 v200, 2, v200\n"
      "s_mov_b64 exec, 0xff\n"
      "s_waitcnt vmcnt(5)\n"
      "buffer_store_dwordx2 v[200:201], v198, s[76:79], 0 offen sc1\n"
      "s_mov_b64 exec, -1\n"
      "v_or_b32 v200, 4, v200\n"
      "s_mov_b64 exec, 0xff\n"
      "s_waitcnt vmcnt(1)\n"
      "buffer_store_dwordx2 v[200:201], v198, s[76:79], 0 offen sc1\n"
      "s_mov_b64 exec, -1\n"
      "s_endpgm\n"
      ".LTLB_%=:\n"
      "s_lshl_b32 s88, s55, 3\n"
      "s_lshr_b32 s88, 0x6f604000, s88\n"
      "s_and_b32 s88, s88, 0xff\n"
      "s_lshl_b32 s88, s88, 14\n"
      "s_lshl_b32 s89, s55, 21\n"
      "s_mov_b32 s90, 0x800000\n"
      "s_waitcnt lgkmcnt(0)\n"
      "s_load_dword s80, s[4:5], s88\n"
      "s_load_dword s81, s[6:7], s88\n"
      "s_load_dword s82, s[8:9], s88\n"
      "s_load_dword s83, s[2:3], s88\n"
      "s_load_dword s84, s[10:11], s88\n"
      "s_load_dword s85, s[12:13], s89\n"
      "s_load_dword s86, s[12:13], s90\n"
      "s_endpgm\n"
      ".LL_IDLE_%=:\n"
      "s_waitcnt lgkmcnt(0)\n"
      "s_and_b32 s44, s19, 3\n"
      "s_lshl_b32 s47, s55, 12\n"
      "s_mov_b64 s[28:29], s[8:9]\n"
      "s_mov_b32 s30, 0x1c0000\n"
      "s_mov_b32 s31, 0x20000\n"
      "s_mov_b64 s[68:69], s[10:11]\n"
      "s_mov_b32 s70, 0x200000\n"
      "s_mov_b32 s71, 0x20000\n"
      "s_mov_b64 s[76:77], s[12:13]\n"
      "s_mov_b32 s78, 0x820000\n"
      "s_mov_b32 s79, 0x20000\n"
      "s_and_b32 s18, s14, 7\n"
      "s_lshr_b32 s44, s19, 3\n"
      "s_lshl2_add_u32 s50, s18, s44\n"
      "s_and_b32 s51, s19, 7\n"
      "s_mul_i32 s48, s55, 0x70000\n"
      "s_lshl_b32 s42, s51, 7\n"
      "s_add_u32 s48, s48, s42\n"
      "s_lshl2_add_u32 s42, s50, s55\n"
      "s_mul_i32 s49, s42, 0xe000\n"
      "s_and_b32 s42, s51, 3\n"
      "s_mul_i32 s42, s42, 2\n"
      "s_and_b32 s44, s44, 1\n"
      "s_mul_i32 s43, s44, 3\n"
      "s_add_u32 s60, s42, s43\n"
      "s_mul_i32 s42, s60, 37\n"
      "s_lshr_b32 s42, s42, 8\n"
      "s_mul_i32 s42, s42, 7\n"
      "s_sub_u32 s60, s60, s42\n"
      "s_add_u32 s61, s60, 1\n"
      "s_cmp_eq_u32 s61, 7\n"
      "s_cselect_b32 s61, 0, s61\n"
      "s_add_u32 s62, s61, 1\n"
      "s_cmp_eq_u32 s62, 7\n"
      "s_cselect_b32 s62, 0, s62\n"
      "s_add_u32 s63, s62, 1\n"
      "s_cmp_eq_u32 s63, 7\n"
      "s_cselect_b32 s63, 0, s63\n"
      "s_add_u32 s64, s63, 1\n"
      "s_cmp_eq_u32 s64, 7\n"
      "s_cselect_b32 s64, 0, s64\n"
      "s_add_u32 s65, s64, 1\n"
      "s_cmp_eq_u32 s65, 7\n"
      "s_cselect_b32 s65, 0, s65\n"
      "s_add_u32 s66, s65, 1\n"
      "s_cmp_eq_u32 s66, 7\n"
      "s_cselect_b32 s66, 0, s66\n"
      "v_lshlrev_b32 v10, 4, v31\n"
      "v_lshl_or_b32 v10, v30, 12, v10\n"
      "v_xor_b32 v11, 32, v10\n"
      "v_xor_b32 v12, 64, v10\n"
      "v_xor_b32 v13, 96, v10\n"
      "s_lshl3_add_u32 s42, s50, s51\n"
      "s_lshl2_add_u32 s42, s42, s55\n"
      "s_lshl_b32 s42, s42, 7\n"
      "s_add_u32 s42, s42, 0x800000\n"
      "v_mov_b32 v14, s42\n"
      "s_add_u32 s74, s12, s42\n"
      "s_addc_u32 s75, s13, 0\n"
      "v_lshlrev_b32 v15, 4, v1\n"
      "v_bfe_u32 v27, v1, 2, 2\n"
      "v_lshrrev_b32 v29, 4, v1\n"
      "v_lshl_add_u32 v29, v27, 3, v29\n"
      "v_lshlrev_b32 v29, 7, v29\n"
      "v_lshl_or_b32 v29, v27, 5, v29\n"
      "v_and_b32 v27, 3, v1\n"
      "v_lshl_or_b32 v29, v27, 3, v29\n"
      "v_add_u32 v16, s47, v29\n"
      "v_xor_b32 v17, 32, v16\n"
      "v_xor_b32 v18, 64, v16\n"
      "v_xor_b32 v19, 96, v16\n"
      "v_add_u32 v20, 0x10000, v16\n"
      "v_add_u32 v21, 0x10000, v17\n"
      "v_add_u32 v22, 0x10000, v18\n"
      "v_add_u32 v23, 0x10000, v19\n"
      "v_add_u32 v24, 0x20000, v16\n"
      "v_add_u32 v25, 0x20000, v17\n"
      "v_add_u32 v26, 0x20000, v18\n"
      "v_add_u32 v27, 0x20000, v19\n"
      "v_and_b32 v2, 31, v1\n"
      "v_lshlrev_b32 v2, 10, v2\n"
      "s_add_u32 m0, s47, 0x10000\n"
      "s_lshl_b32 s43, s60, 16\n"
      "s_add_u32 s43, s43, s48\n"
      "s_add_u32 s43, s43, 0x8000\n"
      "buffer_load_dwordx4 v10, s[28:31], s43 offen lds\n"
      "buffer_load_dwordx4 v11, s[28:31], s43 offen offset:1024 lds\n"
      "buffer_load_dwordx4 v12, s[28:31], s43 offen offset:2048 lds\n"
      "buffer_load_dwordx4 v13, s[28:31], s43 offen offset:3072 lds\n"
      "s_xor_b32 s43, s43, 0x200\n"
      "buffer_load_dword v4, v2, s[28:31], s43 offen\n"
      "s_add_u32 m0, s47, 0x14000\n"
      "s_lshl_b32 s43, s61, 16\n"
      "s_add_u32 s43, s43, s48\n"
      "s_add_u32 s43, s43, 0x8000\n"
      "buffer_load_dwordx4 v10, s[28:31], s43 offen lds\n"
      "buffer_load_dwordx4 v11, s[28:31], s43 offen offset:1024 lds\n"
      "buffer_load_dwordx4 v12, s[28:31], s43 offen offset:2048 lds\n"
      "buffer_load_dwordx4 v13, s[28:31], s43 offen offset:3072 lds\n"
      "s_xor_b32 s43, s43, 0x200\n"
      "buffer_load_dword v4, v2, s[28:31], s43 offen\n"
      "s_mov_b32 m0, s47\n"
      "s_lshl_b32 s43, s62, 16\n"
      "s_add_u32 s43, s43, s48\n"
      "s_add_u32 s43, s43, 0x8000\n"
      "buffer_load_dwordx4 v10, s[28:31], s43 offen lds\n"
      "buffer_load_dwordx4 v11, s[28:31], s43 offen offset:1024 lds\n"
      "buffer_load_dwordx4 v12, s[28:31], s43 offen offset:2048 lds\n"
      "buffer_load_dwordx4 v13, s[28:31], s43 offen offset:3072 lds\n"
      "s_xor_b32 s43, s43, 0x200\n"
      "buffer_load_dword v4, v2, s[28:31], s43 offen\n"
      "s_add_u32 m0, s47, 0xc000\n"
      "s_lshl_b32 s43, s63, 16\n"
      "s_add_u32 s43, s43, s48\n"
      "s_add_u32 s43, s43, 0x8000\n"
      "buffer_load_dwordx4 v10, s[28:31], s43 offen lds\n"
      "buffer_load_dwordx4 v11, s[28:31], s43 offen offset:1024 lds\n"
      "buffer_load_dwordx4 v12, s[28:31], s43 offen offset:2048 lds\n"
      "buffer_load_dwordx4 v13, s[28:31], s43 offen offset:3072 lds\n"
      "s_xor_b32 s43, s43, 0x200\n"
      "buffer_load_dword v4, v2, s[28:31], s43 offen\n"
      "s_add_u32 m0, s47, 0x20000\n"
      "s_lshl_b32 s43, s64, 16\n"
      "s_add_u32 s43, s43, s48\n"
      "s_add_u32 s43, s43, 0x8000\n"
      "buffer_load_dwordx4 v10, s[28:31], s43 offen lds\n"
      "buffer_load_dwordx4 v11, s[28:31], s43 offen offset:1024 lds\n"
      "buffer_load_dwordx4 v12, s[28:31], s43 offen offset:2048 lds\n"
      "buffer_load_dwordx4 v13, s[28:31], s43 offen offset:3072 lds\n"
      "s_xor_b32 s43, s43, 0x200\n"
      "buffer_load_dword v4, v2, s[28:31], s43 offen\n"
      "s_add_u32 m0, s47, 0x24000\n"
      "s_lshl_b32 s43, s65, 16\n"
      "s_add_u32 s43, s43, s48\n"
      "s_add_u32 s43, s43, 0x8000\n"
      "buffer_load_dwordx4 v10, s[28:31], s43 offen lds\n"
      "buffer_load_dwordx4 v11, s[28:31], s43 offen offset:1024 lds\n"
      "buffer_load_dwordx4 v12, s[28:31], s43 offen offset:2048 lds\n"
      "buffer_load_dwordx4 v13, s[28:31], s43 offen offset:3072 lds\n"
      "s_add_u32 m0, s47, 0x8000\n"
      "s_lshl_b32 s43, s66, 16\n"
      "s_add_u32 s43, s43, s48\n"
      "s_add_u32 s43, s43, 0x8000\n"
      "buffer_load_dwordx4 v10, s[28:31], s43 offen lds\n"
      "buffer_load_dwordx4 v11, s[28:31], s43 offen offset:1024 lds\n"
      "buffer_load_dwordx4 v12, s[28:31], s43 offen offset:2048 lds\n"
      "buffer_load_dwordx4 v13, s[28:31], s43 offen offset:3072 lds\n"
      "s_add_u32 m0, s47, 0x4000\n"
      "s_lshl_b32 s43, s60, 16\n"
      "s_add_u32 s43, s43, s48\n"
      "buffer_load_dwordx4 v10, s[28:31], s43 offen lds\n"
      "buffer_load_dwordx4 v11, s[28:31], s43 offen offset:1024 lds\n"
      "buffer_load_dwordx4 v12, s[28:31], s43 offen offset:2048 lds\n"
      "buffer_load_dwordx4 v13, s[28:31], s43 offen offset:3072 lds\n"
      "s_add_u32 m0, s47, 0x18000\n"
      "s_lshl_b32 s43, s61, 16\n"
      "s_add_u32 s43, s43, s48\n"
      "buffer_load_dwordx4 v10, s[28:31], s43 offen lds\n"
      "buffer_load_dwordx4 v11, s[28:31], s43 offen offset:1024 lds\n"
      "buffer_load_dwordx4 v12, s[28:31], s43 offen offset:2048 lds\n"
      "buffer_load_dwordx4 v13, s[28:31], s43 offen offset:3072 lds\n"
      "s_add_u32 m0, s47, 0x1c000\n"
      "s_lshl_b32 s43, s62, 16\n"
      "s_add_u32 s43, s43, s48\n"
      "buffer_load_dwordx4 v10, s[28:31], s43 offen lds\n"
      "buffer_load_dwordx4 v11, s[28:31], s43 offen offset:1024 lds\n"
      "buffer_load_dwordx4 v12, s[28:31], s43 offen offset:2048 lds\n"
      "buffer_load_dwordx4 v13, s[28:31], s43 offen offset:3072 lds\n"
      "s_waitcnt vmcnt(8)\n"
      "ds_read_b64_tr_b16 v[32:33], v16 offset:16384\n"
      "ds_read_b64_tr_b16 v[34:35], v16 offset:16896\n"
      "ds_read_b64_tr_b16 v[36:37], v17 offset:16384\n"
      "ds_read_b64_tr_b16 v[38:39], v17 offset:16896\n"
      "ds_read_b64_tr_b16 v[40:41], v18 offset:16384\n"
      "ds_read_b64_tr_b16 v[42:43], v18 offset:16896\n"
      "ds_read_b64_tr_b16 v[44:45], v19 offset:16384\n"
      "ds_read_b64_tr_b16 v[46:47], v19 offset:16896\n"
      "s_waitcnt lgkmcnt(0)\n"
      "s_add_u32 m0, s47, 0x4000\n"
      "s_lshl_b32 s43, s63, 16\n"
      "s_add_u32 s43, s43, s48\n"
      "buffer_load_dwordx4 v10, s[28:31], s43 offen lds\n"
      "buffer_load_dwordx4 v11, s[28:31], s43 offen offset:1024 lds\n"
      "buffer_load_dwordx4 v12, s[28:31], s43 offen offset:2048 lds\n"
      "buffer_load_dwordx4 v13, s[28:31], s43 offen offset:3072 lds\n"
      "s_waitcnt vmcnt(8)\n"
      "ds_read_b64_tr_b16 v[48:49], v20 offset:32768\n"
      "ds_read_b64_tr_b16 v[50:51], v20 offset:33280\n"
      "ds_read_b64_tr_b16 v[52:53], v21 offset:32768\n"
      "ds_read_b64_tr_b16 v[54:55], v21 offset:33280\n"
      "ds_read_b64_tr_b16 v[56:57], v22 offset:32768\n"
      "ds_read_b64_tr_b16 v[58:59], v22 offset:33280\n"
      "ds_read_b64_tr_b16 v[60:61], v23 offset:32768\n"
      "ds_read_b64_tr_b16 v[62:63], v23 offset:33280\n"
      "s_waitcnt lgkmcnt(0)\n"
      "s_add_u32 m0, s47, 0x18000\n"
      "s_lshl_b32 s43, s64, 16\n"
      "s_add_u32 s43, s43, s48\n"
      "buffer_load_dwordx4 v10, s[28:31], s43 offen lds\n"
      "buffer_load_dwordx4 v11, s[28:31], s43 offen offset:1024 lds\n"
      "buffer_load_dwordx4 v12, s[28:31], s43 offen offset:2048 lds\n"
      "buffer_load_dwordx4 v13, s[28:31], s43 offen offset:3072 lds\n"
      "s_waitcnt vmcnt(8)\n"
      "ds_read_b64_tr_b16 v[64:65], v20 offset:49152\n"
      "ds_read_b64_tr_b16 v[66:67], v20 offset:49664\n"
      "ds_read_b64_tr_b16 v[68:69], v21 offset:49152\n"
      "ds_read_b64_tr_b16 v[70:71], v21 offset:49664\n"
      "ds_read_b64_tr_b16 v[72:73], v22 offset:49152\n"
      "ds_read_b64_tr_b16 v[74:75], v22 offset:49664\n"
      "ds_read_b64_tr_b16 v[76:77], v23 offset:49152\n"
      "ds_read_b64_tr_b16 v[78:79], v23 offset:49664\n"
      "s_waitcnt lgkmcnt(0)\n"
      "s_add_u32 m0, s47, 0x1c000\n"
      "s_lshl_b32 s43, s65, 16\n"
      "s_add_u32 s43, s43, s48\n"
      "buffer_load_dwordx4 v10, s[28:31], s43 offen lds\n"
      "buffer_load_dwordx4 v11, s[28:31], s43 offen offset:1024 lds\n"
      "buffer_load_dwordx4 v12, s[28:31], s43 offen offset:2048 lds\n"
      "buffer_load_dwordx4 v13, s[28:31], s43 offen offset:3072 lds\n"
      "s_waitcnt vmcnt(8)\n"
      "ds_read_b64_tr_b16 v[80:81], v16 offset:16384\n"
      "ds_read_b64_tr_b16 v[82:83], v16 offset:16896\n"
      "ds_read_b64_tr_b16 v[84:85], v17 offset:16384\n"
      "ds_read_b64_tr_b16 v[86:87], v17 offset:16896\n"
      "ds_read_b64_tr_b16 v[88:89], v18 offset:16384\n"
      "ds_read_b64_tr_b16 v[90:91], v18 offset:16896\n"
      "ds_read_b64_tr_b16 v[92:93], v19 offset:16384\n"
      "ds_read_b64_tr_b16 v[94:95], v19 offset:16896\n"
      "s_waitcnt lgkmcnt(0)\n"
      "s_add_u32 m0, s47, 0x4000\n"
      "s_lshl_b32 s43, s66, 16\n"
      "s_add_u32 s43, s43, s48\n"
      "buffer_load_dwordx4 v10, s[28:31], s43 offen lds\n"
      "buffer_load_dwordx4 v11, s[28:31], s43 offen offset:1024 lds\n"
      "buffer_load_dwordx4 v12, s[28:31], s43 offen offset:2048 lds\n"
      "buffer_load_dwordx4 v13, s[28:31], s43 offen offset:3072 lds\n"
      "s_waitcnt vmcnt(8)\n"
      "ds_read_b64_tr_b16 v[96:97], v20 offset:32768\n"
      "ds_read_b64_tr_b16 v[98:99], v20 offset:33280\n"
      "ds_read_b64_tr_b16 v[100:101], v21 offset:32768\n"
      "ds_read_b64_tr_b16 v[102:103], v21 offset:33280\n"
      "ds_read_b64_tr_b16 v[104:105], v22 offset:32768\n"
      "ds_read_b64_tr_b16 v[106:107], v22 offset:33280\n"
      "ds_read_b64_tr_b16 v[108:109], v23 offset:32768\n"
      "ds_read_b64_tr_b16 v[110:111], v23 offset:33280\n"
      "s_waitcnt vmcnt(4)\n"
      "ds_read_b64_tr_b16 v[112:113], v20 offset:49152\n"
      "ds_read_b64_tr_b16 v[114:115], v20 offset:49664\n"
      "ds_read_b64_tr_b16 v[116:117], v21 offset:49152\n"
      "ds_read_b64_tr_b16 v[118:119], v21 offset:49664\n"
      "ds_read_b64_tr_b16 v[120:121], v22 offset:49152\n"
      "ds_read_b64_tr_b16 v[122:123], v22 offset:49664\n"
      "ds_read_b64_tr_b16 v[124:125], v23 offset:49152\n"
      "ds_read_b64_tr_b16 v[126:127], v23 offset:49664\n"
      "s_waitcnt vmcnt(0)\n"
      "ds_read_b64_tr_b16 v[128:129], v16 offset:16384\n"
      "ds_read_b64_tr_b16 v[130:131], v16 offset:16896\n"
      "ds_read_b64_tr_b16 v[132:133], v17 offset:16384\n"
      "ds_read_b64_tr_b16 v[134:135], v17 offset:16896\n"
      "ds_read_b64_tr_b16 v[136:137], v18 offset:16384\n"
      "ds_read_b64_tr_b16 v[138:139], v18 offset:16896\n"
      "ds_read_b64_tr_b16 v[140:141], v19 offset:16384\n"
      "ds_read_b64_tr_b16 v[142:143], v19 offset:16896\n"
      "s_branch .LL_P2_%=\n"
      ".LEND_%=:\n"
      : "+{v0}"(tid), "+{s[0:1]}"(karg), "+{s2}"(bid)
      :
      :
        "v1", "v2", "v3", "v4", "v5", "v6", "v7", "v8", "v9", "v10", "v11", "v12", "v13", "v14", "v15", "v16",
        "v17", "v18", "v19", "v20", "v21", "v22", "v23", "v24", "v25", "v26", "v27", "v28", "v29", "v30", "v31", "v32",
        "v33", "v34", "v35", "v36", "v37", "v38", "v39", "v40", "v41", "v42", "v43", "v44", "v45", "v46", "v47", "v48",
        "v49", "v50", "v51", "v52", "v53", "v54", "v55", "v56", "v57", "v58", "v59", "v60", "v61", "v62", "v63", "v64",
        "v65", "v66", "v67", "v68", "v69", "v70", "v71", "v72", "v73", "v74", "v75", "v76", "v77", "v78", "v79", "v80",
        "v81", "v82", "v83", "v84", "v85", "v86", "v87", "v88", "v89", "v90", "v91", "v92", "v93", "v94", "v95", "v96",
        "v97", "v98", "v99", "v100", "v101", "v102", "v103", "v104", "v105", "v106", "v107", "v108", "v109", "v110", "v111", "v112",
        "v113", "v114", "v115", "v116", "v117", "v118", "v119", "v120", "v121", "v122", "v123", "v124", "v125", "v126", "v127", "v128",
        "v129", "v130", "v131", "v132", "v133", "v134", "v135", "v136", "v137", "v138", "v139", "v140", "v141", "v142", "v143", "v144",
        "v145", "v146", "v147", "v148", "v149", "v150", "v151", "v152", "v153", "v154", "v155", "v156", "v157", "v158", "v159", "v160",
        "v161", "v162", "v163", "v164", "v165", "v166", "v167", "v168", "v169", "v170", "v171", "v172", "v173", "v174", "v175", "v176",
        "v177", "v178", "v179", "v180", "v181", "v182", "v183", "v184", "v185", "v186", "v187", "v188", "v189", "v190", "v191", "v192",
        "v193", "v194", "v195", "v196", "v197", "v198", "v199", "v200", "v201", "v202", "v203", "v204", "v205", "v206", "v207", "v208",
        "v209", "v210", "v211", "v212", "v213", "v214", "v215", "v216", "v217", "v218", "v219", "v220", "v221", "v222", "v223", "v224",
        "v225", "v226", "v227", "v228", "v229", "v230", "v231", "v232", "v233", "v234", "v235", "v236", "v237", "v238", "v239", "v240",
        "v241", "v242", "v243", "v244", "v245", "v246", "v247", "v248", "v249", "v250", "v251", "v252", "v253", "v254", "v255", "s3",
        "s4", "s5", "s6", "s7", "s8", "s9", "s10", "s11", "s12", "s13", "s14", "s15", "s16", "s17", "s18", "s19",
        "s20", "s21", "s22", "s23", "s24", "s25", "s26", "s27", "s28", "s29", "s30", "s31", "s34", "s35", "s36", "s37",
        "s38", "s39", "s40", "s41", "s42", "s43", "s44", "s45", "s46", "s47", "s48", "s49", "s50", "s51", "s52", "s53",
        "s54", "s55", "s56", "s57", "s58", "s59", "s60", "s61", "s62", "s63", "s64", "s65", "s66", "s67", "s68", "s69",
        "s70", "s71", "s72", "s73", "s74", "s75", "s76", "s77", "s78", "s79", "s80", "s81", "s82", "s83", "s84", "s85",
        "s86", "s87", "s88", "s89", "s90", "s91", "s92", "s93", "s94", "s95", "vcc", "scc", "memory");
#endif
}

extern "C" void kernel_launch(void* const* d_in, const int* in_sizes, int n_in, void* d_out, int out_size, void* d_ws,
                              size_t ws_size, hipStream_t stream) {
  (void)hipFuncSetAttribute((const void*)geglu_fused, hipFuncAttributeMaxDynamicSharedMemorySize, LDS_BYTES);
  geglu_fused<<<256, 512, LDS_BYTES, stream>>>(d_in[0], d_in[1], d_in[2], d_in[3], d_out, d_ws);
}
```
